```python
import torch

def _softmax(x, axis=-1):
    return torch.softmax(x, dim=axis)

def _gelu(x):
    return torch.nn.functional.gelu(x, approximate='tanh')

def _silu(x):
    return torch.nn.functional.silu(x)

def _sigmoid(x):
    return torch.sigmoid(x)

def _layernorm(x, axis=-1, eps=1e-05):
    m = x.mean(dim=axis, keepdim=True)
    v = x.var(dim=axis, keepdim=True, unbiased=False)
    return (x - m) / torch.sqrt(v + eps)

def _scatter_add(tgt, idx, src, axis=0):
    return tgt.clone().index_add_(axis, idx, src)

def _segment_sum(src, seg, n):
    out = torch.zeros((n,) + src.shape[1:], dtype=src.dtype)
    return out.index_add_(0, seg, src)

def _pad(x, pw):
    flat = tuple((v for pair in reversed(pw) for v in pair))
    return torch.nn.functional.pad(x, flat)
kernel_name = 'concat_heads__ssm_conv_s4__memory_aug_recurrent_mem_tr__prompt'

def setup_inputs():

    _w = lambda a: (lambda t: t.to(torch.bfloat16) if t.is_floating_point() else t)(torch.as_tensor(a))

    def _A_setup():
        B, L, D, N = (1, 2048, 640, 128)
        A_re = -(lambda _x: torch.exp(torch.as_tensor(_x)))((lambda *s: torch.randn(*s, dtype=torch.bfloat16) * 6.4)(D, N))
        A_im = torch.pi * (lambda *s: torch.randn(*s, dtype=torch.bfloat16) * 0.05)(D, N)
        B_re = (lambda *s: torch.randn(*s, dtype=torch.bfloat16) * 0.1)(D, N)
        B_im = (lambda *s: torch.randn(*s, dtype=torch.bfloat16) * 0.1)(D, N)
        C_re = (lambda *s: torch.randn(*s, dtype=torch.bfloat16) * 0.1)(D, N)
        C_im = (lambda *s: torch.randn(*s, dtype=torch.bfloat16) * 0.1)(D, N)
        log_dt = (lambda *s: torch.randn(*s, dtype=torch.bfloat16) * 0.05)(D) * 0.5 - 4.0
        Dskip = (lambda *s: torch.randn(*s, dtype=torch.bfloat16) * 0.05)(D)
        return {'u': _w((lambda *s: torch.randn(*s, dtype=torch.bfloat16) * 0.05)(B, L, D)), 'A_re': _w(A_re), 'A_im': _w(A_im), 'B_re': _w(B_re), 'B_im': _w(B_im), 'C_re': _w(C_re), 'C_im': _w(C_im), 'log_dt': _w(log_dt), 'Dskip': _w(Dskip)}
    _A_in = _A_setup()

    def _B_setup():
        B, S, Dm = (1, 2048, 640)
        H, Dh = (32, 128)
        M, seg = (8, 32)
        seg = min(seg, S)
        S = S // seg * seg
        return {'x': _w((lambda *s: torch.randn(*s, dtype=torch.bfloat16) * 0.05)(B, S, Dm)), 'mem0': _w((lambda *s: torch.randn(*s, dtype=torch.bfloat16) * 0.4)(B, M, Dm)), 'Wq': _w((lambda *s: torch.randn(*s, dtype=torch.bfloat16) * 6.4)(Dm, H, Dh) * Dm ** (-0.5)), 'Wk': _w((lambda *s: torch.randn(*s, dtype=torch.bfloat16) * 6.4)(Dm, H, Dh) * Dm ** (-0.5)), 'Wv': _w((lambda *s: torch.randn(*s, dtype=torch.bfloat16) * 0.4)(Dm, H, Dh) * Dm ** (-0.5)), 'Wo': _w((lambda *s: torch.randn(*s, dtype=torch.bfloat16) * 0.4)(H, Dh, Dm) * (H * Dh) ** (-0.5)), 'seg_len': seg}
    _B_in = _B_setup()
    return {'A_u': _A_in['u'], 'A_A_re': _A_in['A_re'], 'A_A_im': _A_in['A_im'], 'A_B_re': _A_in['B_re'], 'A_B_im': _A_in['B_im'], 'A_C_re': _A_in['C_re'], 'A_C_im': _A_in['C_im'], 'A_log_dt': _A_in['log_dt'], 'A_Dskip': _A_in['Dskip'], 'B_x': _B_in['x'], 'B_mem0': _B_in['mem0'], 'B_Wq': _B_in['Wq'], 'B_Wk': _B_in['Wk'], 'B_Wv': _B_in['Wv'], 'B_Wo': _B_in['Wo'], 'B_seg_len': _B_in['seg_len']}

def reference(A_u, A_A_re, A_A_im, A_B_re, A_B_im, A_C_re, A_C_im, A_log_dt, A_Dskip, B_x, B_mem0, B_Wq, B_Wk, B_Wv, B_Wo, B_seg_len):

    def _A_ref(u, A_re, A_im, B_re, B_im, C_re, C_im, log_dt, Dskip):
        Bsz, L, D = u.shape
        dt = torch.exp(log_dt)[:, None]
        A = A_re + 1j * A_im
        Bc = B_re + 1j * B_im
        Cc = C_re + 1j * C_im
        logdA = dt * A
        dB = dt * Bc
        l = (lambda x, dt: x.to(dt))(torch.arange(L), A_re.dtype)
        P = torch.exp(logdA[:, :, None] * l[None, None, :])
        K = torch.einsum('dn,dnl,dn->dl', Cc, P, dB).real
        L2 = 2 * L
        Kp = _pad(K, ((0, 0), (0, L)))
        up = (lambda x, *d: x.permute(*d))(u, 0, 2, 1)
        up = _pad(up, ((0, 0), (0, 0), (0, L)))
        Kf = (lambda x, axes=(-2, -1): torch.fft.rfft2(x.float(), dim=axes))(Kp[:, None, :], axes=(-2, -1))
        Uf = (lambda x, axes=(-2, -1): torch.fft.rfft2(x.float(), dim=axes))(up[:, :, None, :], axes=(-2, -1))
        Yf = Uf * Kf[None, :, :, :]
        y = (lambda x, s, axes=(-2, -1): torch.fft.irfft2(x, s=s, dim=axes).to(torch.bfloat16))(Yf, (1, L2), axes=(-2, -1))[:, :, 0, :L]
        y = (lambda x, *d: x.permute(*d))(y, 0, 2, 1) + u * Dskip[None, None, :]
        return y

    def _B_ref(x, mem0, Wq, Wk, Wv, Wo, seg_len):
        B, S, Dm = x.shape
        H, Dh = (Wq.shape[1], Wq.shape[2])
        M = mem0.shape[1]
        scale = Dh ** (-0.5)
        n_seg = S // seg_len
        L = M + seg_len
        idx = torch.arange(L)
        mask = (idx[:, None] < M) | (idx[:, None] >= idx[None, :])
        mem = mem0
        outs = []
        for s in range(n_seg):
            xs = x[:, s * seg_len:(s + 1) * seg_len, :].clone()
            h = torch.concatenate([mem, xs], axis=1)
            q = torch.einsum('btd,dhe->bhte', h, Wq)
            k = torch.einsum('btd,dhe->bhte', h, Wk)
            v = torch.einsum('btd,dhe->bhte', h, Wv)
            scores = torch.einsum('bhqe,bhke->bhqk', q, k) * scale
            scores = torch.where(mask[None, None], scores, -1000000000.0)
            p = _softmax(scores, axis=-1)
            o = torch.einsum('bhqk,bhke->bhqe', p, v)
            o = torch.einsum('bhte,hed->btd', o, Wo)
            mem = o[:, :M, :].clone()
            outs.append(o[:, M:, :])
        return torch.concatenate(outs, axis=1)
    a = _A_ref(A_u, A_A_re, A_A_im, A_B_re, A_B_im, A_C_re, A_C_im, A_log_dt, A_Dskip)
    b = _B_ref(B_x, B_mem0, B_Wq, B_Wk, B_Wv, B_Wo, B_seg_len)
    return torch.concatenate([a, b], axis=-1)
```

```cpp
constexpr int D_X = 0;
constexpr int D_RING = 40960;
constexpr int D_MEM = D_RING + 3 * 16384;
constexpr int D_T = D_MEM + 10240;
constexpr int D_END = D_T + 4096;

DEV void d_ahead(const Params& P, __amdgpu_buffer_rsrc_t rs, int h, int s, int slot, const u32x8 (&wq)[10], const u32x8 (&wk)[10], char* lds, int tid) {
    const int lane = tid & 63, w = tid >> 6, n = lane & 15, g = lane >> 4;
    const char* ximg = lds + D_X;
    f32x4 aq[2] = {{0, 0, 0, 0}, {0, 0, 0, 0}}, ak[2] = {{0, 0, 0, 0}, {0, 0, 0, 0}};
#pragma unroll
    for (int kk = 0; kk < 10; ++kk)
#pragma unroll
        for (int T = 0; T < 2; ++T) {
            const u32x4 b0 = x_frag(ximg, T, 2 * kk, lane), b1 = x_frag(ximg, T, 2 * kk + 1, lane);
            aq[T] = mfma16(lo4(wq[kk]), b0, aq[T]); ak[T] = mfma16(lo4(wk[kk]), b0, ak[T]);
            aq[T] = mfma16(hi4(wq[kk]), b1, aq[T]); ak[T] = mfma16(hi4(wk[kk]), b1, ak[T]);
        }
    char* ring = lds + D_RING + slot * 16384;
#pragma unroll
    for (int T = 0; T < 2; ++T) {
        const u32x2 kv = {pk_bf16(ak[T][0], ak[T][1]), pk_bf16(ak[T][2], ak[T][3])}, qv = {pk_bf16(aq[T][0], aq[T][1]), pk_bf16(aq[T][2], aq[T][3])};
        st8_sc1(rs, WS_KX + (s * NH + h) * 8192 + ((T * 4 + (w >> 1)) * 64 + (2 * (w & 1) + (g >> 1)) * 16 + n) * 16 + 8 * (g & 1), kv);
        const int row = 16 * T + n, sl = 2 * w + (g >> 1);
        lds_st8(ring + row * 256 + ((sl ^ (row & 15)) * 16) + 8 * (g & 1), qv);
        lds_st8(ring + 8192 + row * 256 + ((sl ^ (row & 15)) * 16) + 8 * (g & 1), kv);
    }
}

DEV void d_behind(__amdgpu_buffer_rsrc_t rs, int h, int s, int slot, const u32x8 (&wk)[10], char* lds, int tid) {
    const int lane = tid & 63, w = __builtin_amdgcn_readfirstlane(tid >> 6), n = lane & 15, g = lane >> 4;
    const int sidx = (n < 8) ? 0x4444 : 0xEEEE;
    f32x4 ak = {0, 0, 0, 0}, ak2 = {0, 0, 0, 0};
#pragma unroll
    for (int kk = 0; kk < 5; ++kk) {
        ak = smfma(lds_ld16(lds + D_MEM + (2 * kk) * 1024 + lane * 16), wk[2 * kk], ak, sidx);
        ak2 = smfma(lds_ld16(lds + D_MEM + (2 * kk + 1) * 1024 + lane * 16), wk[2 * kk + 1], ak2, sidx);
    }
    ak += ak2;
    {
        float k0 = ak[0], k1 = ak[1], k2 = ak[2], k3 = ak[3];
        swap32(k0, k2); swap32(k1, k3);
        const int e = 16 * w + n, ro = ((g & 1) * 4 + (g >> 1) * 2) * 2;
        lds_st4(lds + D_T + e * 32 + 16 + ro, pk_bf16(k0 + k2, k1 + k3));
    }
    __syncthreads();
    if (w < 2) {
        const int qt = w;
        const char* ring = lds + D_RING + slot * 16384;
        f32x4 t0 = {0, 0, 0, 0}, t1 = {0, 0, 0, 0}, t2 = {0, 0, 0, 0};
        const int q4 = n >> 2, p = lane & 3;
#pragma unroll
        for (int t = 0; t < 4; ++t) {
            const char* b = lds + D_T + (32 * t + 8 * g + q4) * 32 + 16 + (p & 1) * 8;
            const u32x2 ka = tr_read(b), kb = tr_read(b + 128);
            const u32x4 kf = {ka.x, ka.y, kb.x, kb.y};
            const int sl = 4 * t + g;
            const int rq = 16 * qt + n;
            const u32x4 qf = lds_ld16(ring + rq * 256 + ((sl ^ (rq & 15)) * 16));
            const u32x4 x0 = lds_ld16(ring + 8192 + n * 256 + ((sl ^ n) * 16)), x1 = lds_ld16(ring + 8192 + (16 + n) * 256 + ((sl ^ n) * 16));
            t0 = mfma16(kf, qf, t0); t1 = mfma16(x0, qf, t1); t2 = mfma16(x1, qf, t2);
        }
        const float scale = 0.08838834764831845f, NEG = -1.0e9f;
        const int qi = 16 * qt + n;
        float v[12];
#pragma unroll
        for (int i = 0; i < 4; ++i) { v[i] = t0[i]; v[4 + i] = t1[i]; v[8 + i] = t2[i]; }
#pragma unroll
        for (int i = 0; i < 12; i += 2) {
            u32 a = pk_bf16(v[i], v[i + 1]);
            a = pk_bf16(bf_lo(a) * scale, bf_hi(a) * scale);
            v[i] = bf_lo(a); v[i + 1] = bf_hi(a);
        }
#pragma unroll
        for (int i = 0; i < 4; ++i) {
            v[i] = g < 2 ? v[i] : NEG;
            v[4 + i] = (4 * g + i <= qi) ? v[4 + i] : NEG;
            v[8 + i] = (16 + 4 * g + i <= qi) ? v[8 + i] : NEG;
        }
        float m = vmax3(vmax3(vmax3(v[0], v[1], v[2]), vmax3(v[3], v[4], v[5]), vmax3(v[6], v[7], v[8])), vmax3(v[9], v[10], v[11]), v[0]);
        { float a = m, b = m; swap16(a, b); m = vmax(a, b); }
        { float a = m, b = m; swap32(a, b); m = vmax(a, b); }
        const float ml = -m * 1.4426950408889634f;
        float sum = 0.f;
#pragma unroll
        for (int i = 0; i < 12; ++i) { v[i] = __builtin_amdgcn_exp2f(__builtin_fmaf(v[i], 1.4426950408889634f, ml)); sum += v[i]; }
        { float a = sum, b = sum; swap16(a, b); sum = a + b; }
        { float a = sum, b = sum; swap32(a, b); sum = a + b; }
        const float inv = __builtin_amdgcn_rcpf(sum);
        const u32 pb = WS_PX + s * 81920;
        st16_sc1(rs, pb + (h * 2 + qt) * 1024 + lane * 16,
                 u32x4{pk_bf16(v[4] * inv, v[5] * inv), pk_bf16(v[6] * inv, v[7] * inv), pk_bf16(v[8] * inv, v[9] * inv), pk_bf16(v[10] * inv, v[11] * inv)});
        if (g < 2) st8_sc1(rs, pb + 65536 + ((h >> 2) * 2 + qt) * 1024 + ((h & 3) * 16 + n) * 16 + g * 8, u32x2{pk_bf16(v[0] * inv, v[1] * inv), pk_bf16(v[2] * inv, v[3] * inv)});
    }
}

DEV void f_task(const Params& P, __amdgpu_buffer_rsrc_t rs, int s, int jg, char* lds, int tid) {
    const int lane = tid & 63, w = __builtin_amdgcn_readfirstlane(tid >> 6), n = lane & 15, g = lane >> 4;
    const int j = 8 * jg + w;
    const u32 vb = WS_VW + (u32)(s * 40 + j) * NH * 1024 + lane * 16, zb = WS_ZP + (u32)(s * 40 + j) * NH * 256 + lane * 16;
    u32x4 a[16];
#pragma unroll
    for (int i = 0; i < 16; ++i) a[i] = ld16_sc1(rs, vb + i * 1024);
#pragma unroll
    for (int i = 0; i < 10; ++i) lds_st16(lds + (i * 512 + tid) * 16, ld16_sc1(rs, WS_PX + s * 81920 + (i * 512 + tid) * 16));
    __syncthreads();
    f32x4 d0 = {0, 0, 0, 0}, d1 = {0, 0, 0, 0};
#pragma unroll
    for (int i = 0; i < 16; ++i) {
        d0 = mfma16(a[i], lds_ld16(lds + (i * 2 + 0) * 1024 + lane * 16), d0); d1 = mfma16(a[i], lds_ld16(lds + (i * 2 + 1) * 1024 + lane * 16), d1);
        a[i] = ld16_sc1(rs, vb + (16 + i) * 1024);
    }
    u32x4 z[8];
#pragma unroll
    for (int i = 0; i < 8; ++i) z[i] = ld16_sc1(rs, zb + i * 1024);
#pragma unroll
    for (int i = 0; i < 16; ++i) { d0 = mfma16(a[i], lds_ld16(lds + ((16 + i) * 2 + 0) * 1024 + lane * 16), d0); d1 = mfma16(a[i], lds_ld16(lds + ((16 + i) * 2 + 1) * 1024 + lane * 16), d1); }
#pragma unroll
    for (int i = 0; i < 8; ++i) { d0 = mfma16(z[i], lds_ld16(lds + 65536 + (i * 2 + 0) * 1024 + lane * 16), d0); d1 = mfma16(z[i], lds_ld16(lds + 65536 + (i * 2 + 1) * 1024 + lane * 16), d1); }
    char* o = lds + 81920;
    lds_st8(o + n * 272 + w * 32 + g * 8, u32x2{pk_bf16(d0[0], d0[1]), pk_bf16(d0[2], d0[3])});
    lds_st8(o + (16 + n) * 272 + w * 32 + g * 8, u32x2{pk_bf16(d1[0], d1[1]), pk_bf16(d1[2], d1[3])});
    __syncthreads();
    { const int row = tid >> 4, c = tid & 15;
      *(GAS u32x4*)(P.out + (size_t)(32 * s + row) * OUTW + 640 + 128 * jg + c * 8) = lds_ld16(o + row * 272 + c * 16); }
    __syncthreads();
}

__global__ void __launch_bounds__(256) k_init(Params P) {
    char* ws = ws_base(P.ws);
    const int gt = blockIdx.x * 256 + threadIdx.x, nthr = gridDim.x * 256;
    const u32x4 sv = {SENT, SENT, SENT, SENT};
    for (u32 i = gt; i < (2 * PG + 147456) / 16; i += nthr) {
        const u32 off = i * 16;
        if (off < 32768) {
            const u32 blk = off >> 14, c = blk * 512 + ((off & 8191) >> 4);
            u32x4 v = sv;
            if (c < 640) { const int j = c >> 4, r16 = c & 15, r = r16 & 7, hi = r16 >> 3;
                const u16* m = P.mem0 + r * 640 + 16 * j + 2 * hi;
                v.x = *(const u32*)(m); v.y = *(const u32*)(m + 4); v.z = *(const u32*)(m + 8); v.w = *(const u32*)(m + 12);
                v.x = canon(v.x); v.y = canon(v.y); v.z = canon(v.z); v.w = canon(v.w); }
            *(u32x4*)(ws + off) = v;
        } else *(u32x4*)(ws + off) = sv;
    }
    if (gt < 1024) ((u32*)(ws + WS_CTL))[gt] = 0;
}
__global__ void __launch_bounds__(64) k_calib(Params P) {
    char* ws = ws_base(P.ws);
    const int page = blockIdx.x;
    const unsigned xcc = (unsigned)__builtin_amdgcn_s_getreg(20 | (3 << 11)) & 0xfu;
    char* pb = ws + (size_t)page * PG;
    u32 lat[2] = {0, 0};
    for (int r = -1; r < 6; ++r) for (int par = 0; par < 2; ++par) {
        gu32* p = (gu32*)(pb + PG - 16384 + par * 8192 + (r + 1) * 256);
        unsigned long long t0, t1; u32 v;
        asm volatile("s_waitcnt vmcnt(0) lgkmcnt(0)\n\ts_memtime %0\n\ts_waitcnt lgkmcnt(0)\n\tglobal_load_dword %2, %3, off sc1\n\ts_waitcnt vmcnt(0)\n\ts_memtime %1\n\ts_waitcnt lgkmcnt(0)" : "=&s"(t0), "=&s"(t1), "=&v"(v) : "v"(p) : "memory");
        if (r >= 0) lat[par] += (u32)(t1 - t0);
    }
    u32 nearpar = lat[1] < lat[0] ? 1u : 0u;
    if (xcc >= 4) nearpar ^= 1u;
    if (threadIdx.x == 0) ((u32*)(ws + WS_CTL))[CT_PAR + page] = nearpar;
}

__global__ void __launch_bounds__(512, 2) k_prep(Params P, u16* qx_dbg) {
    extern __shared__ __attribute__((aligned(16))) char lds[];
    char* ws = ws_base(P.ws);
    const int b = blockIdx.x, tid = threadIdx.x;
    if (b < 128) role_c(P, ws, b & 31, lds, tid, (b >> 5) * 16, (b >> 5) * 16 + 16);
    else role_d_ahead(P, ws, b & 31, lds, tid, ((b - 128) >> 5) * 16, ((b - 128) >> 5) * 16 + 16, 1, qx_dbg);
}

__global__ void __launch_bounds__(512, 2) k_chain(Params P) {
    extern __shared__ __attribute__((aligned(16))) char lds[];
    char* ws = ws_base(P.ws);
    gu32* ctl = (gu32*)(ws + WS_CTL);
    const int tid = threadIdx.x;
    const unsigned xcc = (unsigned)__builtin_amdgcn_s_getreg(20 | (3 << 11)) & 0xfu;
    if (xcc >= 4) return;
    if (tid == 0) *(LAS int*)lds = __hip_atomic_fetch_add(ctl + CT_TICK, 1u, RLX_AGENT);
    __syncthreads();
    const int t = __builtin_amdgcn_readfirstlane(*(LAS int*)lds);
    __syncthreads();
    if (t < 32) role_a(P, ws, t, lds, tid);
    else if (t < 64) role_b(P, ws, t - 32, lds, tid);
    else if (t < 104) role_r(P, ws, t - 64, lds, tid);
}

__global__ void __launch_bounds__(512, 2) k_parta(Params P) {
    extern __shared__ __attribute__((aligned(16))) char lds[];
    parta_task(P, blockIdx.x, lds, threadIdx.x);
}

__global__ void __launch_bounds__(512, 2) k_post1(Params P) {
    extern __shared__ __attribute__((aligned(16))) char lds[];
    char* ws = ws_base(P.ws);
    const int tid = threadIdx.x, h = blockIdx.x & 31, part = blockIdx.x >> 5;
    u32x8 wq[10], wk[10];
    load_qk(P.wq, P.wk, h, wq, wk, lds, tid);
    const __amdgpu_buffer_rsrc_t rs = mk_rsrc(ws, WS_BYTES);
    gu32* ctl = (gu32*)(ws + WS_CTL);
    const u32 parM = __hip_atomic_load(ctl + CT_PAR + 0, RLX_AGENT);
    for (int s = part * 8; s < part * 8 + 8; ++s) {
        u32x4 xr[5]; x_issue(P.x, s, xr, tid); x_write(lds + D_X, xr, tid);
        bool first; gather_mem(rs, s, parM, lds + D_MEM, tid, ctl, first);
        __syncthreads();
        d_ahead(P, rs, h, s, 0, wq, wk, lds, tid);
        __syncthreads();
        d_behind(rs, h, s, 0, wk, lds, tid);
        __syncthreads();
    }
}
__global__ void __launch_bounds__(512, 2) k_post2(Params P) {
    extern __shared__ __attribute__((aligned(16))) char lds[];
    char* ws = ws_base(P.ws);
    const __amdgpu_buffer_rsrc_t rs = mk_rsrc(ws, WS_BYTES);
    f_task(P, rs, blockIdx.x / 5, blockIdx.x % 5, lds, threadIdx.x);
}

extern "C" void kernel_launch(void* const* d_in, const int* in_sizes, int n_in, void* d_out, int out_size, void* d_ws, size_t ws_size, hipStream_t stream) {
    Params P;
    P.u = (const u16*)d_in[0]; P.are = (const u16*)d_in[1]; P.aim = (const u16*)d_in[2]; P.bre = (const u16*)d_in[3]; P.bim = (const u16*)d_in[4];
    P.cre = (const u16*)d_in[5]; P.cim = (const u16*)d_in[6]; P.logdt = (const u16*)d_in[7]; P.dskip = (const u16*)d_in[8];
    P.x = (const u16*)d_in[9]; P.mem0 = (const u16*)d_in[10]; P.wq = (const u16*)d_in[11]; P.wk = (const u16*)d_in[12]; P.wv = (const u16*)d_in[13]; P.wo = (const u16*)d_in[14];
    P.out = (u16*)d_out; P.ws = (char*)d_ws;
    P.dA = 0; P.dB = 0; P.dZ = 0; P.dP = 0; P.dec = 0; P.inc = 0;
    constexpr int LDSB = 128 * 1024;
    hipFuncSetAttribute((const void*)k_prep, hipFuncAttributeMaxDynamicSharedMemorySize, LDSB);
    hipFuncSetAttribute((const void*)k_chain, hipFuncAttributeMaxDynamicSharedMemorySize, LDSB);
    hipLaunchKernelGGL(k_init, dim3(512), dim3(256), 0, stream, P);
    hipLaunchKernelGGL(k_calib, dim3(3), dim3(64), 0, stream, P);
    hipLaunchKernelGGL(k_prep, dim3(256), dim3(512), LDSB, stream, P, (u16*)nullptr);
    hipFuncSetAttribute((const void*)k_parta, hipFuncAttributeMaxDynamicSharedMemorySize, 140 * 1024);
    hipLaunchKernelGGL(k_parta, dim3(80), dim3(512), 140 * 1024, stream, P);
    hipLaunchKernelGGL(k_chain, dim3(256), dim3(512), LDSB, stream, P);
    hipFuncSetAttribute((const void*)k_post1, hipFuncAttributeMaxDynamicSharedMemorySize, LDSB);
    hipFuncSetAttribute((const void*)k_post2, hipFuncAttributeMaxDynamicSharedMemorySize, LDSB);
    hipLaunchKernelGGL(k_post1, dim3(256), dim3(512), LDSB, stream, P);
    hipLaunchKernelGGL(k_post2, dim3(320), dim3(512), LDSB, stream, P);
}
```

```cpp
struct cf { float r, i; };
DEV cf cmul(cf a, cf b) { return cf{__builtin_fmaf(a.r, b.r, -a.i * b.i), __builtin_fmaf(a.r, b.i, a.i * b.r)}; }
DEV cf cexp_k(float lr, float rh, float rl, float k) {
    const float mag = __builtin_amdgcn_exp2f(lr * k * 1.4426950408889634f);
    const float fr = __builtin_amdgcn_fractf(__builtin_amdgcn_fractf(rh * k) + rl * k);
    return cf{mag * __builtin_amdgcn_cosf(fr), mag * __builtin_amdgcn_sinf(fr)};
}
constexpr int A_WR = 16896;
constexpr int A_ROW = 528;
constexpr int A_TAB = 0;
constexpr int A_KC0 = 3072;
constexpr int A_KC1 = 3072 + 4224 + 64;
constexpr int A_UT = 3072 + 4224 + 64 + 4224;
constexpr int A_ZERO = A_UT + 4608;
static_assert(A_ZERO + 128 <= A_WR, "part A region");

DEV void parta_task(const Params& P, int task, char* lds, int tid) {
    const int lane = tid & 63, w = __builtin_amdgcn_readfirstlane(tid >> 6);
    const int d0 = task * 8, d = d0 + w;
    char* reg = lds + w * A_WR;
    u32x4 ur[4];
#pragma unroll
    for (int i = 0; i < 4; ++i) ur[i] = *(const GAS u32x4*)(P.u + (size_t)(i * 512 + tid) * DM + d0);
    const float dt = rbf(expf(bf2f(P.logdt[d])));
    f32x4 acc[3] = {{0, 0, 0, 0}, {0, 0, 0, 0}, {0, 0, 0, 0}};
    const int fa = lane & 15, kq = lane >> 4;
#pragma unroll 1
    for (int half = 0; half < 2; ++half) {
        const int n = d * NST + half * 64 + lane;
        const float lr = dt * bf2f(P.are[n]), li = dt * bf2f(P.aim[n]);
        const float br = dt * bf2f(P.bre[n]), bi = dt * bf2f(P.bim[n]);
        const cf W = cmul(cf{bf2f(P.cre[n]), bf2f(P.cim[n])}, cf{br, bi});
        const float rh = li * 0.15915494309189535f;
        const float rl = __builtin_fmaf(li, 0.15915494309189535f, -rh) + li * 6.4206383167e-9f;
        const cf E1 = cexp_k(lr, rh, rl, 1.f), E8 = cexp_k(lr, rh, rl, 8.f), E128 = cexp_k(lr, rh, rl, 128.f);
        char* Lt = reg; char* Rt = reg + 16 * A_ROW;
        {
            cf r = {1.f, 0.f};
#pragma unroll
            for (int j = 0; j < 16; ++j) { *(LAS f32x2*)(Rt + j * A_ROW + lane * 8) = f32x2{r.r, -r.i}; r = cmul(r, E8); }
        }
        cf C[8]; C[0] = cf{1.f, 0.f};
#pragma unroll
        for (int c = 1; c < 8; ++c) C[c] = cmul(C[c - 1], E1);
        f32x4 rb[8];
#pragma unroll
        for (int G = 0; G < 8; ++G) rb[G] = *(const LAS f32x4*)(Rt + fa * A_ROW + (8 * G + 2 * kq) * 8);
        cf WB = W;
#pragma unroll
        for (int T = 0; T < 3; ++T) {
            if (T == 0) {
#pragma unroll
                for (int a = 0; a < 2; ++a) {
#pragma unroll
                    for (int c = 0; c < 8; ++c) { const cf v = c ? cmul(WB, C[c]) : WB; *(LAS f32x2*)(Lt + (8 * a + c) * A_ROW + lane * 8) = f32x2{v.r, v.i}; }
                    WB = cmul(WB, E128);
                }
            } else if (T == 1) {
#pragma unroll
                for (int a = 0; a < 2; ++a) {
#pragma unroll
                    for (int c = 0; c < 4; ++c) { const cf v = c ? cmul(WB, C[2 * c]) : WB; *(LAS f32x2*)(Lt + (4 * a + c) * A_ROW + lane * 8) = f32x2{v.r, v.i}; }
                    WB = cmul(WB, E128);
                }
#pragma unroll
                for (int a = 0; a < 4; ++a) {
#pragma unroll
                    for (int c = 0; c < 2; ++c) { const cf v = c ? cmul(WB, C[4]) : WB; *(LAS f32x2*)(Lt + (8 + 2 * a + c) * A_ROW + lane * 8) = f32x2{v.r, v.i}; }
                    WB = cmul(WB, E128);
                }
            } else {
#pragma unroll
                for (int a = 0; a < 9; ++a) { *(LAS f32x2*)(Lt + a * A_ROW + lane * 8) = f32x2{WB.r, WB.i}; WB = cmul(WB, E128); }
#pragma unroll
                for (int a = 9; a < 16; ++a) *(LAS f32x2*)(Lt + a * A_ROW + lane * 8) = f32x2{0.f, 0.f};
            }
#pragma unroll
            for (int G = 0; G < 8; ++G) {
                const f32x4 la = *(const LAS f32x4*)(Lt + fa * A_ROW + (8 * G + 2 * kq) * 8);
#pragma unroll
                for (int e = 0; e < 4; ++e) acc[T] = __builtin_amdgcn_mfma_f32_16x16x4f32(la[e], rb[G][e], acc[T], 0, 0, 0);
            }
        }
    }
#pragma unroll
    for (int T = 0; T < 3; ++T)
#pragma unroll
        for (int i = 0; i < 4; ++i) *(LAS float*)(reg + A_TAB + ((16 * T + 4 * kq + i) * 16 + fa) * 4) = acc[T][i];
    auto kval = [&](int l) -> float {
        const int lb = (int)rbf((float)l);
        const int sh = min(max(24 - (int)__builtin_clz((unsigned)lb | 1u), 0), 3);
        const int row = 8 * sh + ((lb >> 7) << (3 - sh)) + ((lb & 7) >> sh);
        return *(const LAS float*)(reg + A_TAB + (row * 16 + ((lb >> 3) & 15)) * 4);
    };
#pragma unroll 4
    for (int it = 0; it < 16; ++it) {
        const int p = it * 64 + lane, l0 = 2047 - 2 * p;
        lds_st4(reg + A_KC0 + p * 4, pk_bf16(kval(l0), kval(l0 - 1)));
    }
    if (lane < 33) lds_st4(reg + A_KC0 + (1024 + lane) * 4, 0u);
#pragma unroll 4
    for (int it = 0; it < 17; ++it) {
        const int p = min(it * 64 + lane, 1055);
        const u32 a = *(const LAS u32*)(reg + A_KC0 + p * 4), b2 = *(const LAS u32*)(reg + A_KC0 + p * 4 + 4);
        lds_st4(reg + A_KC1 + p * 4, __builtin_amdgcn_alignbit(b2, a, 16));
    }
    if (lane < 32) lds_st4(reg + A_ZERO + lane * 4, 0u);
    __syncthreads();
#pragma unroll
    for (int i = 0; i < 4; ++i) {
        const int t = i * 512 + tid;
#pragma unroll
        for (int c = 0; c < 4; ++c) {
            *(LAS u16*)(lds + (2 * c) * A_WR + A_UT + (t >> 6) * 144 + (t & 63) * 2) = (u16)ur[i][c];
            *(LAS u16*)(lds + (2 * c + 1) * A_WR + A_UT + (t >> 6) * 144 + (t & 63) * 2) = (u16)(ur[i][c] >> 16);
        }
    }
    __syncthreads();
    f32x16 y0, y1;
#pragma unroll
    for (int i = 0; i < 16; ++i) { y0[i] = 0.f; y1[i] = 0.f; }
    const int b = lane & 31, hh = lane >> 5;
    const char* kbase = reg + ((b & 1) ? A_KC0 + (2047 - b + 8 * hh) * 2 : A_KC1 + (2046 - b + 8 * hh) * 2);
    auto kfrag = [&](int m) -> u32x4 { const char* p = kbase - 32 * m; u32x4 r; r.x = *(const LAS u32*)(p); r.y = *(const LAS u32*)(p + 4); r.z = *(const LAS u32*)(p + 8); r.w = *(const LAS u32*)(p + 12); return r; };
    u32x4 Fm1 = kfrag(-1), Fm2 = kfrag(-2), Fm3 = kfrag(-3);
#pragma unroll 2
    for (int dl = 0; dl < 32; ++dl) {
        const u32x4 F0 = kfrag(4 * dl), F1 = kfrag(4 * dl + 1), F2 = kfrag(4 * dl + 2);
        const char* ub = (b >= dl) ? reg + A_UT + (b - dl) * 144 + 16 * hh : reg + A_ZERO;
        const int ks = (b >= dl) ? 32 : 0;
        const u32x4 U0 = lds_ld16(ub), U1 = lds_ld16(ub + ks), U2 = lds_ld16(ub + 2 * ks), U3 = lds_ld16(ub + 3 * ks);
        y0 = mfma32(F0, U0, y0); y1 = mfma32(F2, U0, y1);
        y0 = mfma32(Fm1, U1, y0); y1 = mfma32(F1, U1, y1);
        y0 = mfma32(Fm2, U2, y0); y1 = mfma32(F0, U2, y1);
        y0 = mfma32(Fm3, U3, y0); y1 = mfma32(Fm1, U3, y1);
        Fm1 = kfrag(4 * dl + 3); Fm2 = F2; Fm3 = F1;
    }
#pragma unroll
    for (int q = 0; q < 4; ++q) {
        lds_st8(reg + b * 144 + (8 * q + 4 * hh) * 2, u32x2{pk_bf16(y0[4 * q], y0[4 * q + 1]), pk_bf16(y0[4 * q + 2], y0[4 * q + 3])});
        lds_st8(reg + b * 144 + (32 + 8 * q + 4 * hh) * 2, u32x2{pk_bf16(y1[4 * q], y1[4 * q + 1]), pk_bf16(y1[4 * q + 2], y1[4 * q + 3])});
    }
    __syncthreads();
    const u32x4 dsk = *(const GAS u32x4*)(P.dskip + d0);
#pragma unroll
    for (int i = 0; i < 4; ++i) {
        const int t = i * 512 + tid;
        u32x4 yv;
#pragma unroll
        for (int c = 0; c < 4; ++c) {
            const u32 lo = *(const LAS u16*)(lds + (2 * c) * A_WR + (t >> 6) * 144 + (t & 63) * 2), hi = *(const LAS u16*)(lds + (2 * c + 1) * A_WR + (t >> 6) * 144 + (t & 63) * 2);
            yv[c] = lo | (hi << 16);
        }
        u32x4 o;
#pragma unroll
        for (int c = 0; c < 4; ++c) {
            const u32 sk = pk_bf16(bf_lo(ur[i][c]) * bf_lo(dsk[c]), bf_hi(ur[i][c]) * bf_hi(dsk[c]));
            o[c] = pk_bf16(bf_lo(yv[c]) + bf_lo(sk), bf_hi(yv[c]) + bf_hi(sk));
        }
        *(GAS u32x4*)(P.out + (size_t)t * OUTW + d0) = o;
    }
    __syncthreads();
}

__global__ void __launch_bounds__(512, 2) concat_heads_mega(Params P) {
    extern __shared__ __attribute__((aligned(16))) char lds[];
    char* ws = ws_base(P.ws);
    gu32* ctl = (gu32*)P.ws;
    const int tid = threadIdx.x;
    if (tid == 0) {
        const unsigned xcc = (unsigned)__builtin_amdgcn_s_getreg(20 | (3 << 11)) & 0xfu;
        const int half = xcc >= 4;
        int role;
        const int t = (int)__hip_atomic_fetch_add(cw(ctl, half ? CT_TICK1 : CT_TICK0), 1u, RLX_AGENT);
        if (t < (half ? N_HALF1 : N_HALF0)) role = half ? N_HALF0 + t : t;
        else {
            u32 n0 = 0, n1 = 0, spins = 0;
            for (;;) {
                n0 = __hip_atomic_load(cw(ctl, CT_TICK0), RLX_AGENT); n1 = __hip_atomic_load(cw(ctl, CT_TICK1), RLX_AGENT);
                if (n0 + n1 >= N_WG) break;
                __builtin_amdgcn_s_sleep(1);
                if (++spins > SPIN_CAP) { __hip_atomic_store(cw(ctl, CT_ABORT), 1u, RLX_AGENT); break; }
            }
            const int o = (int)__hip_atomic_fetch_add(cw(ctl, CT_OVF), 1u, RLX_AGENT);
            const int miss0 = max(0, N_HALF0 - (int)n0), miss1 = max(0, N_HALF1 - (int)n1);
            if (o < miss0) role = (int)n0 + o;
            else if (o < miss0 + miss1) role = N_HALF0 + (int)n1 + (o - miss0);
            else role = N_HALF0 + N_HALF1 + (o - miss0 - miss1);
        }
        *(LAS int*)lds = role;
    }
    __syncthreads();
    const int role = rfl(*(LAS int*)lds);
    __syncthreads();
    if (role < N_A) role_a(P, ws, ctl, role, lds, tid);
    else if (role < N_A + N_B) role_b(P, ws, ctl, role - N_A, lds, tid);
    else if (role < N_HALF0) role_r(P, ws, ctl, role - N_A - N_B, lds, tid);
    else if (role < N_HALF0 + N_C) role_c(P, ws, ctl, role - N_HALF0, lds, tid);
    else if (role < N_HALF0 + N_HALF1) { const int d = role - N_HALF0 - N_C; role_d(P, ws, ctl, d & 31, d >> 5, lds, tid); }
    else {
        const int wk = role - N_HALF0 - N_HALF1;
        for (int t = wk; t < DM / 8; t += N_W) parta_task(P, t, lds, tid);
        const rsrc_t rs = mk_rsrc(ws, WS_BYTES);
        for (int t = wk; t < NSEG * 5; t += N_W) f_task(P, rs, ctl, t / 5, t % 5, lds, tid);
    }
}

__global__ void __launch_bounds__(512) concat_heads_setup(Params P) {
    __shared__ u32 best[2];
    char* ws = ws_base(P.ws);
    gu32* ctl = (gu32*)P.ws;
    const int tid = threadIdx.x, j = blockIdx.x, w = tid >> 6;
    if (j == 3) {
        for (int i = tid; i < CT_LINES; i += 512) if (i < CT_PAR || i >= CT_PAR + 3) *cw(ctl, i) = 0;
        return;
    }
    if (tid < 2) best[tid] = 0xffffffffu;
    __syncthreads();
    const u32 base = j == 0 ? WS_MEM + 16384 + 4096 : j == 1 ? WS_Z + 120 * 16384 : WS_P + 9 * 16384;
    char* p0 = ws + base + w * 256;
    __hip_atomic_store((gu32*)(p0 + 8192), 0u, RLX_AGENT);
#pragma unroll 1
    for (int i = 0; i < 3; ++i) {
        char* p = p0 + (i == 2 ? 8192 : 0);
        u64 t0, t1; const u32 z = 0;
        asm volatile("s_memtime %0\n\ts_waitcnt lgkmcnt(0)\n\tglobal_store_dword %2, %3, off sc1\n\ts_waitcnt vmcnt(0)\n\ts_memtime %1\n\ts_waitcnt lgkmcnt(0)"
                     : "=&s"(t0), "=&s"(t1) : "v"(p), "v"(z) : "memory");
        if (i > 0 && (tid & 63) == 0) atomicMin(&best[i - 1], (u32)(t1 - t0));
    }
    __syncthreads();
    if (tid == 0) {
        const unsigned xcc = (unsigned)__builtin_amdgcn_s_getreg(20 | (3 << 11)) & 0xfu;
        *cw(ctl, CT_PAR + j) = (best[1] < best[0] ? 1u : 0u) ^ (xcc >= 4 ? 1u : 0u);
    }
}

extern "C" void kernel_launch(void* const* d_in, const int* in_sizes, int n_in, void* d_out, int out_size, void* d_ws, size_t ws_size, hipStream_t stream) {
    Params P;
    P.u = (const u16*)d_in[0]; P.are = (const u16*)d_in[1]; P.aim = (const u16*)d_in[2]; P.bre = (const u16*)d_in[3]; P.bim = (const u16*)d_in[4];
    P.cre = (const u16*)d_in[5]; P.cim = (const u16*)d_in[6]; P.logdt = (const u16*)d_in[7]; P.dskip = (const u16*)d_in[8];
    P.x = (const u16*)d_in[9]; P.mem0 = (const u16*)d_in[10]; P.wq = (const u16*)d_in[11]; P.wk = (const u16*)d_in[12]; P.wv = (const u16*)d_in[13]; P.wo = (const u16*)d_in[14];
    P.out = (u16*)d_out; P.ws = (char*)d_ws;
    (void)hipFuncSetAttribute((const void*)concat_heads_mega, hipFuncAttributeMaxDynamicSharedMemorySize, LDS_BYTES);
    hipLaunchKernelGGL(concat_heads_setup, dim3(4), dim3(512), 0, stream, P);
    hipLaunchKernelGGL(concat_heads_mega, dim3(N_WG), dim3(512), LDS_BYTES, stream, P);
}
```

```cpp
struct cf { float r, i; };
DEV cf cmul(cf a, cf b) { return cf{__builtin_fmaf(a.r, b.r, -a.i * b.i), __builtin_fmaf(a.r, b.i, a.i * b.r)}; }
DEV cf cexp_k(float lr, float rh, float rl, float k) {
    const float mag = __builtin_amdgcn_exp2f(lr * k * 1.4426950408889634f);
    const float fr = __builtin_amdgcn_fractf(__builtin_amdgcn_fractf(rh * k) + rl * k);
    return cf{mag * __builtin_amdgcn_cosf(fr), mag * __builtin_amdgcn_sinf(fr)};
}
constexpr int A_WR = 16896;
constexpr int A_ROW = 528;
constexpr int A_TAB = 0;
constexpr int A_KC0 = 3072;
constexpr int A_KC1 = 3072 + 4224 + 64;
constexpr int A_UT = 3072 + 4224 + 64 + 4224;
constexpr int A_ZERO = A_UT + 4608;
static_assert(A_ZERO + 128 <= A_WR, "part A region");

DEV void parta_task(const Params& P, int task, char* lds, int tid) {
    const int lane = tid & 63, w = __builtin_amdgcn_readfirstlane(tid >> 6);
    const int d0 = task * 8, d = d0 + w;
    char* reg = lds + w * A_WR;
    u32x4 ur[4];
#pragma unroll
    for (int i = 0; i < 4; ++i) ur[i] = *(const GAS u32x4*)(P.u + (size_t)(i * 512 + tid) * DM + d0);
    const float dt = rbf(expf(bf2f(P.logdt[d])));
    f32x4 acc[3] = {{0, 0, 0, 0}, {0, 0, 0, 0}, {0, 0, 0, 0}};
    const int fa = lane & 15, kq = lane >> 4;
#pragma unroll 1
    for (int half = 0; half < 2; ++half) {
        const int n = d * NST + half * 64 + lane;
        const float lr = dt * bf2f(P.are[n]), li = dt * bf2f(P.aim[n]);
        const float br = dt * bf2f(P.bre[n]), bi = dt * bf2f(P.bim[n]);
        const cf W = cmul(cf{bf2f(P.cre[n]), bf2f(P.cim[n])}, cf{br, bi});
        const float rh = li * 0.15915494309189535f;
        const float rl = __builtin_fmaf(li, 0.15915494309189535f, -rh) + li * 6.4206383167e-9f;
        const cf E1 = cexp_k(lr, rh, rl, 1.f), E8 = cexp_k(lr, rh, rl, 8.f), E128 = cexp_k(lr, rh, rl, 128.f);
        char* Lt = reg; char* Rt = reg + 16 * A_ROW;
        {
            cf r = {1.f, 0.f};
#pragma unroll
            for (int j = 0; j < 16; ++j) { *(LAS f32x2*)(Rt + j * A_ROW + lane * 8) = f32x2{r.r, -r.i}; r = cmul(r, E8); }
        }
        cf C[8]; C[0] = cf{1.f, 0.f};
#pragma unroll
        for (int c = 1; c < 8; ++c) C[c] = cmul(C[c - 1], E1);
        f32x4 rb[8];
#pragma unroll
        for (int G = 0; G < 8; ++G) rb[G] = *(const LAS f32x4*)(Rt + fa * A_ROW + (8 * G + 2 * kq) * 8);
        cf WB = W;
#pragma unroll
        for (int T = 0; T < 3; ++T) {
            if (T == 0) {
#pragma unroll
                for (int a = 0; a < 2; ++a) {
#pragma unroll
                    for (int c = 0; c < 8; ++c) { const cf v = c ? cmul(WB, C[c]) : WB; *(LAS f32x2*)(Lt + (8 * a + c) * A_ROW + lane * 8) = f32x2{v.r, v.i}; }
                    WB = cmul(WB, E128);
                }
            } else if (T == 1) {
#pragma unroll
                for (int a = 0; a < 2; ++a) {
#pragma unroll
                    for (int c = 0; c < 4; ++c) { const cf v = c ? cmul(WB, C[2 * c]) : WB; *(LAS f32x2*)(Lt + (4 * a + c) * A_ROW + lane * 8) = f32x2{v.r, v.i}; }
                    WB = cmul(WB, E128);
                }
#pragma unroll
                for (int a = 0; a < 4; ++a) {
#pragma unroll
                    for (int c = 0; c < 2; ++c) { const cf v = c ? cmul(WB, C[4]) : WB; *(LAS f32x2*)(Lt + (8 + 2 * a + c) * A_ROW + lane * 8) = f32x2{v.r, v.i}; }
                    WB = cmul(WB, E128);
                }
            } else {
#pragma unroll
                for (int a = 0; a < 9; ++a) { *(LAS f32x2*)(Lt + a * A_ROW + lane * 8) = f32x2{WB.r, WB.i}; WB = cmul(WB, E128); }
#pragma unroll
                for (int a = 9; a < 16; ++a) *(LAS f32x2*)(Lt + a * A_ROW + lane * 8) = f32x2{0.f, 0.f};
            }
#pragma unroll
            for (int G = 0; G < 8; ++G) {
                const f32x4 la = *(const LAS f32x4*)(Lt + fa * A_ROW + (8 * G + 2 * kq) * 8);
#pragma unroll
                for (int e = 0; e < 4; ++e) acc[T] = __builtin_amdgcn_mfma_f32_16x16x4f32(la[e], rb[G][e], acc[T], 0, 0, 0);
            }
        }
    }
#pragma unroll
    for (int T = 0; T < 3; ++T)
#pragma unroll
        for (int i = 0; i < 4; ++i) *(LAS float*)(reg + A_TAB + ((16 * T + 4 * kq + i) * 16 + fa) * 4) = acc[T][i];
    auto kval = [&](int l) -> float {
        const int lb = (int)rbf((float)l);
        const int sh = min(max(24 - (int)__builtin_clz((unsigned)lb | 1u), 0), 3);
        const int row = 8 * sh + ((lb >> 7) << (3 - sh)) + ((lb & 7) >> sh);
        return *(const LAS float*)(reg + A_TAB + (row * 16 + ((lb >> 3) & 15)) * 4);
    };
#pragma unroll 4
    for (int it = 0; it < 16; ++it) {
        const int p = it * 64 + lane, l0 = 2047 - 2 * p;
        lds_st4(reg + A_KC0 + p * 4, pk_bf16(kval(l0), kval(l0 - 1)));
    }
    if (lane < 33) lds_st4(reg + A_KC0 + (1024 + lane) * 4, 0u);
#pragma unroll 4
    for (int it = 0; it < 17; ++it) {
        const int p = min(it * 64 + lane, 1055);
        const u32 a = *(const LAS u32*)(reg + A_KC0 + p * 4), b2 = *(const LAS u32*)(reg + A_KC0 + p * 4 + 4);
        lds_st4(reg + A_KC1 + p * 4, __builtin_amdgcn_alignbit(b2, a, 16));
    }
    if (lane < 32) lds_st4(reg + A_ZERO + lane * 4, 0u);
    __syncthreads();
#pragma unroll
    for (int i = 0; i < 4; ++i) {
        const int t = i * 512 + tid;
#pragma unroll
        for (int c = 0; c < 4; ++c) {
            *(LAS u16*)(lds + (2 * c) * A_WR + A_UT + (t >> 6) * 144 + (t & 63) * 2) = (u16)ur[i][c];
            *(LAS u16*)(lds + (2 * c + 1) * A_WR + A_UT + (t >> 6) * 144 + (t & 63) * 2) = (u16)(ur[i][c] >> 16);
        }
    }
    __syncthreads();
    f32x16 y0, y1;
#pragma unroll
    for (int i = 0; i < 16; ++i) { y0[i] = 0.f; y1[i] = 0.f; }
    const int b = lane & 31, hh = lane >> 5;
    const char* kbase = reg + ((b & 1) ? A_KC0 + (2047 - b + 8 * hh) * 2 : A_KC1 + (2046 - b + 8 * hh) * 2);
    auto kfrag = [&](int m) -> u32x4 { const char* p = kbase - 32 * m; u32x4 r; r.x = *(const LAS u32*)(p); r.y = *(const LAS u32*)(p + 4); r.z = *(const LAS u32*)(p + 8); r.w = *(const LAS u32*)(p + 12); return r; };
    u32x4 Fm1 = kfrag(-1), Fm2 = kfrag(-2), Fm3 = kfrag(-3);
#pragma unroll 2
    for (int dl = 0; dl < 32; ++dl) {
        const u32x4 F0 = kfrag(4 * dl), F1 = kfrag(4 * dl + 1), F2 = kfrag(4 * dl + 2);
        const char* ub = (b >= dl) ? reg + A_UT + (b - dl) * 144 + 16 * hh : reg + A_ZERO;
        const int ks = (b >= dl) ? 32 : 0;
        const u32x4 U0 = lds_ld16(ub), U1 = lds_ld16(ub + ks), U2 = lds_ld16(ub + 2 * ks), U3 = lds_ld16(ub + 3 * ks);
        y0 = mfma32(F0, U0, y0); y1 = mfma32(F2, U0, y1);
        y0 = mfma32(Fm1, U1, y0); y1 = mfma32(F1, U1, y1);
        y0 = mfma32(Fm2, U2, y0); y1 = mfma32(F0, U2, y1);
        y0 = mfma32(Fm3, U3, y0); y1 = mfma32(Fm1, U3, y1);
        Fm1 = kfrag(4 * dl + 3); Fm2 = F2; Fm3 = F1;
    }
#pragma unroll
    for (int q = 0; q < 4; ++q) {
        lds_st8(reg + b * 144 + (8 * q + 4 * hh) * 2, u32x2{pk_bf16(y0[4 * q], y0[4 * q + 1]), pk_bf16(y0[4 * q + 2], y0[4 * q + 3])});
        lds_st8(reg + b * 144 + (32 + 8 * q + 4 * hh) * 2, u32x2{pk_bf16(y1[4 * q], y1[4 * q + 1]), pk_bf16(y1[4 * q + 2], y1[4 * q + 3])});
    }
    __syncthreads();
    const u32x4 dsk = *(const GAS u32x4*)(P.dskip + d0);
#pragma unroll
    for (int i = 0; i < 4; ++i) {
        const int t = i * 512 + tid;
        u32x4 yv;
#pragma unroll
        for (int c = 0; c < 4; ++c) {
            const u32 lo = *(const LAS u16*)(lds + (2 * c) * A_WR + (t >> 6) * 144 + (t & 63) * 2), hi = *(const LAS u16*)(lds + (2 * c + 1) * A_WR + (t >> 6) * 144 + (t & 63) * 2);
            yv[c] = lo | (hi << 16);
        }
        u32x4 o;
#pragma unroll
        for (int c = 0; c < 4; ++c) {
            const u32 sk = pk_bf16(bf_lo(ur[i][c]) * bf_lo(dsk[c]), bf_hi(ur[i][c]) * bf_hi(dsk[c]));
            o[c] = pk_bf16(bf_lo(yv[c]) + bf_lo(sk), bf_hi(yv[c]) + bf_hi(sk));
        }
        *(GAS u32x4*)(P.out + (size_t)t * OUTW + d0) = o;
    }
    __syncthreads();
}

constexpr int F_FINE0 = 56;

__global__ void __launch_bounds__(512, 2) concat_heads_mega(Params P) {
    extern __shared__ __attribute__((aligned(16))) char lds[];
    char* ws = ws_base(P.ws);
    gu32* ctl = (gu32*)P.ws;
    const int tid = threadIdx.x;
    if (false) {
        const u32 ldst = (u32)(size_t)(LAS char*)lds + LDS_PF;
#pragma unroll
        for (int i = 0; i < 10; ++i) {
            const int p = min((int)(blockIdx.x * 68 + ((tid >> 6) - 1) * 10 + i), 17407);
            const char* src = p < 12288 ? (const char*)(p < 4096 ? P.wq : p < 8192 ? P.wk : P.wv) + (size_t)(1024 + (p & 4095)) * 1024 : (const char*)P.wo + (size_t)(p - 12288) * 1024;
            src += (tid & 63) * 16;
            u32 keep;
            asm volatile("s_mov_b32 %0, m0\n\ts_mov_b32 m0, %2\n\ts_nop 0\n\tglobal_load_lds_dwordx4 %1, off\n\ts_mov_b32 m0, %0" : "=&s"(keep) : "v"(src), "s"(ldst) : "memory");
        }
    }
    if (tid == 0) {
        const unsigned xcc = (unsigned)__builtin_amdgcn_s_getreg(20 | (3 << 11)) & 0xfu;
        const int half = xcc >= 4;
        int role;
        const int t = (int)__hip_atomic_fetch_add(cw(ctl, half ? CT_TICK1 : CT_TICK0), 1u, RLX_AGENT);
        if (t < (half ? N_HALF1 : N_HALF0)) role = half ? N_HALF0 + t : t;
        else {
            u32 n0 = 0, n1 = 0, spins = 0;
            for (;;) {
                n0 = __hip_atomic_load(cw(ctl, CT_TICK0), RLX_AGENT); n1 = __hip_atomic_load(cw(ctl, CT_TICK1), RLX_AGENT);
                if (n0 + n1 >= N_WG) break;
                __builtin_amdgcn_s_sleep(1);
                if (++spins > SPIN_CAP) { __hip_atomic_store(cw(ctl, CT_ABORT), 1u, RLX_AGENT); break; }
            }
            const int o = (int)__hip_atomic_fetch_add(cw(ctl, CT_OVF), 1u, RLX_AGENT);
            const int miss0 = max(0, N_HALF0 - (int)n0), miss1 = max(0, N_HALF1 - (int)n1);
            if (o < miss0) role = (int)n0 + o;
            else if (o < miss0 + miss1) role = N_HALF0 + (int)n1 + (o - miss0);
            else role = N_HALF0 + N_HALF1 + (o - miss0 - miss1);
        }
        *(LAS int*)lds = role;
    }
    __syncthreads();
    const int role = rfl(*(LAS int*)lds);
    __syncthreads();
    if (role < N_A) role_a(P, ws, ctl, role, lds, tid);
    else if (role < N_A + N_B) role_b(P, ws, ctl, role - N_A, lds, tid);
    else if (role < N_HALF0) role_r(P, ws, ctl, role - N_A - N_B, lds, tid);
    else if (role < N_HALF0 + N_C) role_c(P, ws, ctl, role - N_HALF0, lds, tid);
    else if (role < N_HALF0 + N_HALF1) { const int d = role - N_HALF0 - N_C; role_d(P, ws, ctl, d & 31, d >> 5, lds, tid); }
    else {
        const int wk = role - N_HALF0 - N_HALF1;
        for (int t = wk; t < DM / 8; t += N_W) parta_task(P, t, lds, tid);
        const rsrc_t rs = mk_rsrc(ws, WS_BYTES);
        {
            for (int t = wk; t < F_FINE0 * 5; t += N_W) f_task(P, rs, ctl, t / 5, t % 5, lds, tid);
            for (int t = wk; t < (NSEG - F_FINE0) * 40; t += N_W) f_fine(P, rs, ctl, F_FINE0 + t / 40, t % 40, lds, tid);
        }
    }
}

__global__ void __launch_bounds__(512) concat_heads_setup(Params P) {
    __shared__ u32 best[2];
    char* ws = ws_base(P.ws);
    gu32* ctl = (gu32*)P.ws;
    const int tid = threadIdx.x, j = blockIdx.x, w = tid >> 6;
    if (j == 3) {
        for (int i = tid; i < CT_LINES; i += 512) if (i < CT_PAR || i >= CT_PAR + 3) *cw(ctl, i) = 0;
        return;
    }
    if (tid < 2) best[tid] = 0xffffffffu;
    __syncthreads();
    const u32 base = j == 0 ? WS_MEM + 16384 + 4096 : j == 1 ? WS_Z + 120 * 16384 : WS_P + 9 * 16384;
    char* p0 = ws + base + w * 256;
    __hip_atomic_store((gu32*)(p0 + 8192), 0u, RLX_AGENT);
#pragma unroll 1
    for (int i = 0; i < 3; ++i) {
        char* p = p0 + (i == 2 ? 8192 : 0);
        u64 t0, t1; const u32 z = 0;
        asm volatile("s_memtime %0\n\ts_waitcnt lgkmcnt(0)\n\tglobal_store_dword %2, %3, off sc1\n\ts_waitcnt vmcnt(0)\n\ts_memtime %1\n\ts_waitcnt lgkmcnt(0)"
                     : "=&s"(t0), "=&s"(t1) : "v"(p), "v"(z) : "memory");
        if (i > 0 && (tid & 63) == 0) atomicMin(&best[i - 1], (u32)(t1 - t0));
    }
    __syncthreads();
    if (tid == 0) {
        const unsigned xcc = (unsigned)__builtin_amdgcn_s_getreg(20 | (3 << 11)) & 0xfu;
        *cw(ctl, CT_PAR + j) = (best[1] < best[0] ? 1u : 0u) ^ (xcc >= 4 ? 1u : 0u);
    }
}

extern "C" void kernel_launch(void* const* d_in, const int* in_sizes, int n_in, void* d_out, int out_size, void* d_ws, size_t ws_size, hipStream_t stream) {
    Params P;
    P.u = (const u16*)d_in[0]; P.are = (const u16*)d_in[1]; P.aim = (const u16*)d_in[2]; P.bre = (const u16*)d_in[3]; P.bim = (const u16*)d_in[4];
    P.cre = (const u16*)d_in[5]; P.cim = (const u16*)d_in[6]; P.logdt = (const u16*)d_in[7]; P.dskip = (const u16*)d_in[8];
    P.x = (const u16*)d_in[9]; P.mem0 = (const u16*)d_in[10]; P.wq = (const u16*)d_in[11]; P.wk = (const u16*)d_in[12]; P.wv = (const u16*)d_in[13]; P.wo = (const u16*)d_in[14];
    P.out = (u16*)d_out; P.ws = (char*)d_ws;
    (void)hipFuncSetAttribute((const void*)concat_heads_mega, hipFuncAttributeMaxDynamicSharedMemorySize, LDS_BYTES);
    hipLaunchKernelGGL(concat_heads_setup, dim3(4), dim3(512), 0, stream, P);
    hipLaunchKernelGGL(concat_heads_mega, dim3(N_WG), dim3(512), LDS_BYTES, stream, P);
}
```

```cpp
#include <hip/hip_runtime.h>
#include <hip/hip_bf16.h>

typedef unsigned short u16;
typedef unsigned int u32;
typedef unsigned long long u64;
typedef u32 u32x2 __attribute__((ext_vector_type(2)));
typedef u32 u32x4 __attribute__((ext_vector_type(4)));
typedef u32 u32x8 __attribute__((ext_vector_type(8)));
typedef float f32x2 __attribute__((ext_vector_type(2)));
typedef float f32x4 __attribute__((ext_vector_type(4)));
typedef float f32x16 __attribute__((ext_vector_type(16)));
typedef __bf16 bf16x8 __attribute__((ext_vector_type(8)));
typedef __bf16 bf16x16 __attribute__((ext_vector_type(16)));
typedef short s16x4 __attribute__((ext_vector_type(4)));
#define LAS __attribute__((address_space(3)))
#define GAS __attribute__((address_space(1)))
typedef GAS u32 gu32;
#define RLX_AGENT __ATOMIC_RELAXED, __HIP_MEMORY_SCOPE_AGENT
#define DEV __device__ __forceinline__
typedef __amdgpu_buffer_rsrc_t rsrc_t;

constexpr int SEQ = 2048, DM = 640, NH = 32, DH = 128, NMEM = 8, SEG = 32, NSEG = 64, NST = 128;
constexpr int OUTW = 1280;
constexpr int NSTEP = NSEG - 1;

constexpr int CT_ABORT = 0, CT_INIT = 4, CT_PAR = 5  , CT_VW = 8  , CT_PX = 72  , CT_ZP = 136  ,
              CT_KX = 200  , CT_FQ = 264, CT_LINES = 265,
              CT_READY = CT_LINES, CT_SLOT = CT_LINES + 1, CT_ALL = CT_SLOT + 256;
constexpr u32 CTL_BYTES = CT_ALL * 128;
constexpr u32 PG = 2u << 20;
constexpr u32 WS_MEM = 0;
constexpr u32 WS_Z = PG;
constexpr u32 WS_P = 2 * PG;
constexpr u32 WS_CANP = WS_P + 10 * 16384;
constexpr u32 WS_KX = 3 * PG;
constexpr u32 WS_ZP = WS_KX + NSEG * NH * 8192;
constexpr u32 WS_PX = WS_ZP + NSEG * 40 * NH * 256;
constexpr u32 WS_VW = WS_PX + NSEG * 81920;
constexpr u32 WS_QK = WS_VW + NSEG * 40 * NH * 1024;
constexpr u32 WS_END = WS_QK + NH * 16384;
constexpr u32 WS_BYTES = WS_END;
constexpr u32 SENT = 0xFFFFFFFFu;
constexpr u32 SPIN_CAP = 1u << 18;
constexpr int N_A = 32, N_B = 32, N_R = 40, N_C = 32, N_D = 64, N_HALF0 = N_A + N_B + N_R, N_HALF1 = N_C + N_D, N_WG = 256, N_W = N_WG - N_HALF0 - N_HALF1;
constexpr int LDS_SLOT = 135168, LDS_BYTES = LDS_SLOT + 16;

struct Params {
    const u16* u; const u16* are; const u16* aim; const u16* bre; const u16* bim; const u16* cre; const u16* cim; const u16* logdt; const u16* dskip;
    const u16* x; const u16* mem0; const u16* wq; const u16* wk; const u16* wv; const u16* wo;
    u16* out; char* ws;
};

DEV char* ws_base(char* ws) { return (char*)(((size_t)ws + CTL_BYTES + (PG - 1)) & ~(size_t)(PG - 1)); }
DEV rsrc_t mk_rsrc(const void* p, u32 bytes) { return __builtin_amdgcn_make_buffer_rsrc((void*)p, 0, bytes, 0x00020000); }
DEV u32x4 ld16_sc1(rsrc_t r, u32 off) { return __builtin_amdgcn_raw_buffer_load_b128(r, off, 0, 16); }
DEV void st16_sc1(rsrc_t r, u32 off, u32x4 v) { __builtin_amdgcn_raw_buffer_store_b128(v, r, off, 0, 16); }
DEV void st8_sc1(rsrc_t r, u32 off, u32x2 v) { __builtin_amdgcn_raw_buffer_store_b64(v, r, off, 0, 16); }
DEV void st4_sc1(rsrc_t r, u32 off, u32 v) { __builtin_amdgcn_raw_buffer_store_b32(v, r, off, 0, 16); }
DEV void st2_sc1(rsrc_t r, u32 off, u16 v) { __builtin_amdgcn_raw_buffer_store_b16(v, r, off, 0, 16); }
DEV f32x4 mfma16(u32x4 a, u32x4 b, f32x4 c) { return __builtin_amdgcn_mfma_f32_16x16x32_bf16(__builtin_bit_cast(bf16x8, a), __builtin_bit_cast(bf16x8, b), c, 0, 0, 0); }
DEV f32x16 mfma32(u32x4 a, u32x4 b, f32x16 c) { return __builtin_amdgcn_mfma_f32_32x32x16_bf16(__builtin_bit_cast(bf16x8, a), __builtin_bit_cast(bf16x8, b), c, 0, 0, 0); }
DEV f32x4 smfma(u32x4 a, u32x8 b, f32x4 c, int idx) { return __builtin_amdgcn_smfmac_f32_16x16x64_bf16(__builtin_bit_cast(bf16x8, a), __builtin_bit_cast(bf16x16, b), c, idx, 0, 0); }
DEV u32x4 lo4(u32x8 v) { return __builtin_shufflevector(v, v, 0, 1, 2, 3); }
DEV u32x4 hi4(u32x8 v) { return __builtin_shufflevector(v, v, 4, 5, 6, 7); }
DEV u32 pk_bf16(float a, float b) { typedef __bf16 b2 __attribute__((ext_vector_type(2))); f32x2 f = {a, b}; return __builtin_bit_cast(u32, __builtin_convertvector(f, b2)); }
DEV float bf_lo(u32 v) { return __uint_as_float(v << 16); }
DEV float bf_hi(u32 v) { return __uint_as_float(v & 0xffff0000u); }
DEV float bf2f(u16 v) { return __uint_as_float((u32)v << 16); }
DEV float rbf(float a) { return bf_lo(pk_bf16(a, a)); }
DEV u32x2 tr_read(const char* p) { return __builtin_bit_cast(u32x2, __builtin_amdgcn_ds_read_tr16_b64_v4i16((LAS s16x4*)p)); }
DEV u32x4 lds_ld16(const char* p) { return *(const LAS u32x4*)p; }
DEV void lds_st16(char* p, u32x4 v) { *(LAS u32x4*)p = v; }
DEV void lds_st8(char* p, u32x2 v) { *(LAS u32x2*)p = v; }
DEV void lds_st4(char* p, u32 v) { *(LAS u32*)p = v; }
DEV bool is_sent(u32x4 v) { return max(max(v.x, v.y), max(v.z, v.w)) == SENT; }
DEV bool any_sign(u32x4 v) { return ((v.x | v.y | v.z | v.w) & 0x80008000u) != 0; }
DEV u32 canon(u32 v) { return min(v, 0xFFFFFFFEu); }
DEV void swap32(float& a, float& b) { auto r = __builtin_amdgcn_permlane32_swap(__float_as_uint(a), __float_as_uint(b), false, false); a = __uint_as_float(r[0]); b = __uint_as_float(r[1]); }
DEV void swap16(float& a, float& b) { auto r = __builtin_amdgcn_permlane16_swap(__float_as_uint(a), __float_as_uint(b), false, false); a = __uint_as_float(r[0]); b = __uint_as_float(r[1]); }
DEV float ror8(float a) { return __uint_as_float(__builtin_amdgcn_update_dpp(0, __float_as_uint(a), 0x128  , 0xf, 0xf, true)); }
DEV float vmax(float a, float b) { float r; asm("v_max_f32 %0, %1, %2" : "=v"(r) : "v"(a), "v"(b)); return r; }
DEV float vmax3(float a, float b, float c) { float r; asm("v_max3_f32 %0, %1, %2, %3" : "=v"(r) : "v"(a), "v"(b), "v"(c)); return r; }
DEV float vmax3n(float a, float b, float c) { float r; asm("v_max3_f32 %0, %1, %2, %3\n\ts_nop 1" : "=v"(r) : "v"(a), "v"(b), "v"(c)); return r; }
DEV void drain() { asm volatile("s_waitcnt vmcnt(0)" ::: "memory"); }
DEV int opaque(int v) { asm volatile("" : "+v"(v)); return v; }
DEV int rfl(int v) { return __builtin_amdgcn_readfirstlane(v); }
DEV gu32* cw(gu32* ctl, int line) { return ctl + line * 32; }
DEV bool spin_abort(u32& spins, gu32* ctl) {
    if (++spins > SPIN_CAP) { __hip_atomic_store(cw(ctl, CT_ABORT), 1u, RLX_AGENT); return true; }
    if ((spins & 255u) == 0u) return rfl(__hip_atomic_load(cw(ctl, CT_ABORT), RLX_AGENT)) != 0;
    return false;
}
DEV bool wait_ge(gu32* ctl, int line, u32 v) {
    u32 spins = 0;
    while ((u32)rfl(__hip_atomic_load(cw(ctl, line), RLX_AGENT)) < v) { __builtin_amdgcn_s_sleep(2); if (spin_abort(spins, ctl)) return false; }
    return true;
}
DEV bool wait_ge_slow(gu32* ctl, int line, u32 v) {
    u32 spins = 0;
    while ((u32)rfl(__hip_atomic_load(cw(ctl, line), RLX_AGENT)) < v) { __builtin_amdgcn_s_sleep(8); if (spin_abort(spins, ctl)) return false; }
    return true;
}
DEV u32 rt24() { return (u32)__builtin_amdgcn_s_memrealtime() << 8; }
DEV u32 wait_hints8(rsrc_t rs, gu32* ctl, u32 base, int h0, u32 tag, int lane, u32 dA, u32 dB, u32& tB) {
    u32 spins = 0, x;
    const int l = lane & 7;
    const u32 off = base + ((l >> 2) * 32 + h0 + (l & 3)) * 128;
    for (;;) {
        x = __builtin_amdgcn_raw_buffer_load_b32(rs, off, 0, 16);
        if (__all((x & 255u) == tag)) break;
        __builtin_amdgcn_s_sleep(2);
        if (spin_abort(spins, ctl)) break;
    }
    x = (x & ~255u) + ((l >> 2) ? dB : dA);
    const u32 x0 = (u32)rfl((int)x);
    int d = (int)(x - x0);
    d = max(d, __shfl_xor(d, 1)); d = max(d, __shfl_xor(d, 2));
    const int da = __builtin_amdgcn_readlane(d, 0), db = __builtin_amdgcn_readlane(d, 4);
    tB = x0 + (u32)db;
    return x0 + (u32)da;
}
DEV u32 wait_hint1(rsrc_t rs, gu32* ctl, u32 off, u32 tag, bool slow) {
    u32 spins = 0, x;
    for (;;) {
        x = (u32)rfl((int)__builtin_amdgcn_raw_buffer_load_b32(rs, off, 0, 16));
        if ((x & 255u) >= tag) break;
        if (slow) __builtin_amdgcn_s_sleep(16); else __builtin_amdgcn_s_sleep(1);
        if (spin_abort(spins, ctl)) break;
    }
    return x;
}
DEV void wait_rt(u32 t) { while ((int)(rt24() - t) < 0) { } }
DEV void wait_ready(gu32* ctl, u32 epoch) {
    u32 spins = 0;
    while ((u32)rfl(__hip_atomic_load(cw(ctl, CT_READY), RLX_AGENT)) != epoch) { __builtin_amdgcn_s_sleep(1); if (++spins > SPIN_CAP) break; }
}
DEV int swz8(int row) { return (row & 3) | (((row >> 3) & 1) << 2); }

DEV u32x8 tr_frag(const char* img, int rowstride, int slot0, int lane) {
    const int gb = lane >> 4, q = (lane & 15) >> 2, p = lane & 3;
    u32x8 r;
#pragma unroll
    for (int t4 = 0; t4 < 4; ++t4) {
        const int row = 32 * (t4 >> 1) + 8 * gb + 4 * (t4 & 1) + q;
        const int slot = slot0 + (p >> 1);
        const int sl = (slot & ~15) | ((slot & 15) ^ (2 * swz8(row)));
        u32x2 v = tr_read(img + row * rowstride + sl * 16 + 8 * (p & 1));
        r[2 * t4] = v.x; r[2 * t4 + 1] = v.y;
    }
    return r;
}
DEV void load_qk(const u16* Wq, const u16* Wk, int h, u32x8 (&wq)[10], u32x8 (&wk)[10], char* lds, int tid, char* ws = nullptr, int slot = 0) {
    const int lane = tid & 63, w = tid >> 6;
    const rsrc_t rq = mk_rsrc(Wq, 640 * 4096 * 2), rk = mk_rsrc(Wk, 640 * 4096 * 2);
    const u32 voff = ((tid >> 4) * 4096 + h * 128 + (tid & 15) * 8) * 2;
    constexpr int AHEAD = 4;
    u32x4 raw[10][4];
    auto issue = [&](int kk) {
#pragma unroll
        for (int i = 0; i < 4; ++i) raw[kk][i] = __builtin_amdgcn_raw_buffer_load_b128((i >> 1) ? rk : rq, voff, (64 * kk + (i & 1) * 32) * 8192, 0);
        __builtin_amdgcn_sched_barrier(0);
    };
#pragma unroll
    for (int kk = 0; kk < AHEAD; ++kk) { issue(kk); __builtin_amdgcn_s_barrier(); }
    const int row0 = tid >> 4, sl_ = tid & 15;
    char* wbase = lds + row0 * 256 + ((sl_ ^ (2 * swz8(row0))) * 16);
#pragma unroll
    for (int kk = 0; kk < 10; ++kk) {
        char* buf = lds + (kk & 1) * 32768;
        if (kk + AHEAD < 10) issue(kk + AHEAD);
#pragma unroll
        for (int i = 0; i < 4; ++i) lds_st16(wbase + (kk & 1) * 32768 + (i >> 1) * 16384 + (i & 1) * 32 * 256, raw[kk][i]);
        __syncthreads();
        wq[kk] = tr_frag(buf, 256, 2 * w, lane);
        wk[kk] = tr_frag(buf + 16384, 256, 2 * w, lane);
        __builtin_amdgcn_sched_barrier(0);
    }
    __syncthreads();
}
DEV void load_vo(const u16* Wv, const u16* Wo, int h, u32x8 (&wv)[10], u32x8 (&wo)[5][2], char* lds, int tid) {
    const int lane = tid & 63, w = tid >> 6;
    const rsrc_t rv_ = mk_rsrc(Wv, 640 * 4096 * 2), ro_ = mk_rsrc(Wo, 4096 * 640 * 2);
    const u32 voff = ((tid >> 4) * 4096 + h * 128 + (tid & 15) * 8) * 2;
    u32x4 rv[10][2], ro[4][5];
    auto issue_v = [&](int kk) {
#pragma unroll
        for (int i = 0; i < 2; ++i) rv[kk][i] = __builtin_amdgcn_raw_buffer_load_b128(rv_, voff, (64 * kk + i * 32) * 8192, 0);
        __builtin_amdgcn_sched_barrier(0);
    };
    auto issue_o = [&](int st) {
#pragma unroll
        for (int i = 0; i < 5; ++i) ro[st][i] = __builtin_amdgcn_raw_buffer_load_b128(ro_, h * 163840 + tid * 16, st * 40960 + i * 8192, 0);
        __builtin_amdgcn_sched_barrier(0);
    };
#pragma unroll
    for (int kk = 0; kk < 8; ++kk) { issue_v(kk); if (kk & 1) __builtin_amdgcn_s_barrier(); }
    const int row0 = tid >> 4, slot0 = tid & 15;
    char* wbase = lds + row0 * 256 + ((slot0 ^ (2 * swz8(row0))) * 16);
#pragma unroll
    for (int kk = 0; kk < 10; ++kk) {
        char* buf = lds + (kk & 1) * 16384;
        if (kk < 2) issue_v(kk + 8);
        else if (kk == 2 || kk == 4 || kk == 6 || kk == 8) issue_o(kk / 2 - 1);
#pragma unroll
        for (int i = 0; i < 2; ++i) lds_st16(wbase + (kk & 1) * 16384 + i * 32 * 256, rv[kk][i]);
        __syncthreads();
        wv[kk] = tr_frag(buf, 256, 2 * w, lane);
        __builtin_amdgcn_sched_barrier(0);
    }
#pragma unroll
    for (int st = 0; st < 4; ++st) {
        char* buf = lds + 32768 + (st & 1) * 40960;
#pragma unroll
        for (int i = 0; i < 5; ++i) {
            const int c = i * 512 + tid, row = c / 80, slot = c % 80;
            const int sl = (slot & ~15) | ((slot & 15) ^ (2 * swz8(row)));
            lds_st16(buf + row * 1280 + sl * 16, ro[st][i]);
        }
        __syncthreads();
        const int gb = lane >> 4, q = (lane & 15) >> 2, p = lane & 3;
#pragma unroll
        for (int jt = 0; jt < 5; ++jt)
#pragma unroll
            for (int t2 = 0; t2 < 2; ++t2) {
                const int row = 8 * gb + 4 * t2 + q;
                const int slot = 2 * (5 * w + jt) + (p >> 1);
                const int sl = (slot & ~15) | ((slot & 15) ^ (2 * swz8(row)));
                u32x2 v = tr_read(buf + row * 1280 + sl * 16 + 8 * (p & 1));
                wo[jt][st >> 1][4 * (st & 1) + 2 * t2] = v.x; wo[jt][st >> 1][4 * (st & 1) + 2 * t2 + 1] = v.y;
            }
        __builtin_amdgcn_sched_barrier(0);
    }
    __syncthreads();
}

DEV void x_issue(const u16* x, int s, u32x4 (&r)[5], int tid) {
    const rsrc_t rx = mk_rsrc(x, SEQ * DM * 2);
    const u32 vo = (tid >> 4) * 1280 + (tid & 15) * 16;
#pragma unroll
    for (int i = 0; i < 5; ++i) r[i] = __builtin_amdgcn_raw_buffer_load_b128(rx, vo + i * 256, s * 40960, 0);
}
DEV void x_write(char* img, const u32x4 (&r)[5], int tid) {
    char* p = img + (tid >> 4) * 1280 + (((tid ^ (tid >> 4)) & 15) * 16);
#pragma unroll
    for (int i = 0; i < 5; ++i) lds_st16(p + i * 256, r[i]);
}
DEV u32x4 x_frag(const char* img, int T, int t, int lane) {
    const int row = 16 * T + (lane & 15), slot = 4 * t + (lane >> 4);
    return lds_ld16(img + row * 1280 + (((slot & ~15) | ((slot ^ row) & 15)) * 16));
}

#define CAL_ROUNDS 2
#define C_EAGER 3
#define F_PACE 4
#define F_SLEEP 4
#define TAIL_DELAY 140
#define TAIL_DELAY2 290
#define R_VWDLY 70
#define B_STDLY 80
#define W_DELAY 800
#define DLY_B 84
#define DLY_R 84
template <int NW, bool PIN = false>
DEV void xgemm(const char* ximg, int lane, const u32x8 (&w0_)[10], const u32x8 (&w1_)[10], f32x4 (&a0)[2], f32x4 (&a1)[2]) {
    u32x4 f[2][4];
#pragma unroll
    for (int i = 0; i < 4; ++i) f[0][i] = x_frag(ximg, i >> 1, i & 1, lane);
#pragma unroll
    for (int kk = 0; kk < 10; ++kk) {
        if (kk < 9) {
#pragma unroll
            for (int i = 0; i < 4; ++i) f[(kk + 1) & 1][i] = x_frag(ximg, i >> 1, 2 * (kk + 1) + (i & 1), lane);
        }
        __builtin_amdgcn_sched_barrier(0);
        const u32x4 (&c)[4] = f[kk & 1];
        u32x8 w0 = w0_[kk], w1 = w1_[kk];
        if (PIN) { asm volatile("" : "+v"(w0)); if (NW > 1) asm volatile("" : "+v"(w1)); }
        a0[0] = mfma16(lo4(w0), c[0], a0[0]); a0[1] = mfma16(lo4(w0), c[2], a0[1]);
        if (NW > 1) { a1[0] = mfma16(lo4(w1), c[0], a1[0]); a1[1] = mfma16(lo4(w1), c[2], a1[1]); }
        a0[0] = mfma16(hi4(w0), c[1], a0[0]); a0[1] = mfma16(hi4(w0), c[3], a0[1]);
        if (NW > 1) { a1[0] = mfma16(hi4(w1), c[1], a1[0]); a1[1] = mfma16(hi4(w1), c[3], a1[1]); }
        __builtin_amdgcn_sched_barrier(0);
    }
}

DEV void role_c(const Params& P, char* ws, gu32* ctl, u32 epoch, int h, char* lds, int tid) {
    const int lane = tid & 63, w = tid >> 6, n = lane & 15, g = lane >> 4;
    u32x8 wv[10], wo[5][2];
    u32x4 xr[5];
    x_issue(P.x, 0, xr, tid);
    load_vo(P.wv, P.wo, h, wv, wo, lds, tid);
    wait_ready(ctl, epoch);
    const rsrc_t rs = mk_rsrc(ws, WS_BYTES);
    char* ximg = lds;
    char* vimg = lds + 40960;
    for (int s = 0; s < NSEG; ++s) {

        x_write(ximg, xr, tid);
        __syncthreads();
        f32x4 acc[2] = {{0, 0, 0, 0}, {0, 0, 0, 0}};
        xgemm<1>(ximg, lane, wv, wv, acc, acc);
#pragma unroll
        for (int T = 0; T < 2; ++T) {
            const int row = 16 * T + n, slot = 2 * w + (g >> 1);
            lds_st8(vimg + row * 256 + ((slot ^ (row & 15)) * 16) + 8 * (g & 1), u32x2{pk_bf16(acc[T][0], acc[T][1]), pk_bf16(acc[T][2], acc[T][3])});
        }
        drain();
        if (s + 1 < NSEG) x_issue(P.x, s + 1, xr, tid);
        __syncthreads();
        if (s > C_EAGER && tid == 0) __hip_atomic_fetch_add(cw(ctl, CT_VW + s - 1), 1u, RLX_AGENT);
        u32x4 va[2][4];
#pragma unroll
        for (int T = 0; T < 2; ++T)
#pragma unroll
            for (int t = 0; t < 4; ++t) { const int row = 16 * T + n, slot = 4 * t + g; va[T][t] = lds_ld16(vimg + row * 256 + ((slot ^ (row & 15)) * 16)); }
#pragma unroll
        for (int jt = 0; jt < 5; ++jt) {
            f32x4 d0 = {0, 0, 0, 0}, d1 = {0, 0, 0, 0};
#pragma unroll
            for (int t = 0; t < 4; ++t) {
                const u32x4 b = (t & 1) ? hi4(wo[jt][t >> 1]) : lo4(wo[jt][t >> 1]);
                d0 = mfma16(va[0][t], b, d0); d1 = mfma16(va[1][t], b, d1);
            }
            st16_sc1(rs, WS_VW + ((s * 40 + 5 * w + jt) * NH + h) * 1024 + lane * 16,
                     u32x4{pk_bf16(d0[0], d0[1]), pk_bf16(d0[2], d0[3]), pk_bf16(d1[0], d1[1]), pk_bf16(d1[2], d1[3])});
        }
        if (s < C_EAGER) drain();
        __syncthreads();
        if (s < C_EAGER && tid == 0) __hip_atomic_fetch_add(cw(ctl, CT_VW + s), 1u, RLX_AGENT);
    }
    drain();
    __syncthreads();
    if (tid == 0) __hip_atomic_fetch_add(cw(ctl, CT_VW + NSEG - 1), 1u, RLX_AGENT);
}

DEV bool gather_mem(rsrc_t rs, int s, u32 par, char* dst, int tid, gu32* ctl, u32& spins, u32& tnow) {
    const int w = rfl(tid >> 6);
    const u32 o0 = WS_MEM + s * 32768 + par * 8192 + tid * 16;
    u32x4 v0, v1 = {0, 0, 0, 0};
    bool ok = true;
    spins = 0;
    for (;;) {
        v0 = ld16_sc1(rs, o0);
        bool bad;
        if (w < 2) { v1 = ld16_sc1(rs, o0 + 16384); bad = is_sent(v0) | is_sent(v1); } else bad = is_sent(v0);
        if (!__any(bad)) break;
        if (spin_abort(spins, ctl)) { ok = false; break; }
    }
    tnow = rt24();
    lds_st16(dst + tid * 16, v0);
    if (w < 2) lds_st16(dst + 8192 + tid * 16, v1);
    return ok;
}
DEV u32 get_par(gu32* ctl, int page) { wait_ge(ctl, CT_PAR + page, 2); return (u32)rfl(__hip_atomic_load(cw(ctl, CT_PAR + page), RLX_AGENT)) & 1u; }
DEV void calibrate(char* ws, gu32* ctl, u32 epoch, int j, char* lds, int tid) {
    const int w = tid >> 6;
    LAS u32* best = (LAS u32*)lds;
    if (tid < 2) best[tid] = 0xffffffffu;
    __syncthreads();
    const u32 base = j == 0 ? WS_MEM + 16384 + 4096 : j == 1 ? WS_Z + 120 * 16384 : WS_P + 9 * 16384;
    char* p0 = ws + base + w * 256;
    __hip_atomic_store((gu32*)(p0 + 8192), 0u, RLX_AGENT);
#pragma unroll 1
    for (int i = 0; i < 1 + 2 * CAL_ROUNDS; ++i) {
        char* p = p0 + ((i && !(i & 1)) ? 8192 : 0);
        u64 t0, t1; const u32 z = 0;
        asm volatile("s_memtime %0\n\ts_waitcnt lgkmcnt(0)\n\tglobal_store_dword %2, %3, off sc1\n\ts_waitcnt vmcnt(0)\n\ts_memtime %1\n\ts_waitcnt lgkmcnt(0)"
                     : "=&s"(t0), "=&s"(t1) : "v"(p), "v"(z) : "memory");
        if (i > 0 && (tid & 63) == 0) __hip_atomic_fetch_min(&best[(i & 1) ^ 1], (u32)(t1 - t0), __ATOMIC_RELAXED, __HIP_MEMORY_SCOPE_WORKGROUP);
    }
    __syncthreads();
    wait_ready(ctl, epoch);
    if (tid == 0) {
        const unsigned xcc = (unsigned)__builtin_amdgcn_s_getreg(20 | (3 << 11)) & 0xfu;
        __hip_atomic_store(cw(ctl, CT_PAR + j), 2u | ((best[1] < best[0] ? 1u : 0u) ^ (xcc >= 4 ? 1u : 0u)), RLX_AGENT);
    }
    __syncthreads();
}

constexpr int D_X = 0;
constexpr int D_RING = 40960;
constexpr int D_MEM = D_RING + 3 * 16384;
constexpr int D_T = D_MEM + 10240;

DEV void d_ahead(rsrc_t rs, int h, int s, int slot, const u32x8 (&wq)[10], const u32x8 (&wk)[10], char* lds, int tid, bool drain_first) {
    f32x4 aq[2] = {{0, 0, 0, 0}, {0, 0, 0, 0}}, ak[2] = {{0, 0, 0, 0}, {0, 0, 0, 0}};
    xgemm<2>(lds + D_X, tid & 63, wq, wk, aq, ak);
    __builtin_amdgcn_sched_barrier(0);
    if (drain_first) drain();
    tid = opaque(tid);
    const int lane = tid & 63, w = tid >> 6, n = lane & 15, g = lane >> 4;
    char* ring = lds + D_RING + slot * 16384;
#pragma unroll
    for (int T = 0; T < 2; ++T) {
        const u32x2 kv = {pk_bf16(ak[T][0], ak[T][1]), pk_bf16(ak[T][2], ak[T][3])}, qv = {pk_bf16(aq[T][0], aq[T][1]), pk_bf16(aq[T][2], aq[T][3])};
        st8_sc1(rs, WS_KX + (s * NH + h) * 8192 + ((T * 4 + (w >> 1)) * 64 + (2 * (w & 1) + (g >> 1)) * 16 + n) * 16 + 8 * (g & 1), kv);
        const int row = 16 * T + n, sl = 2 * w + (g >> 1);
        lds_st8(ring + row * 256 + ((sl ^ (row & 15)) * 16) + 8 * (g & 1), qv);
        lds_st8(ring + 8192 + row * 256 + ((sl ^ (row & 15)) * 16) + 8 * (g & 1), kv);
    }
}
DEV void d_behind(rsrc_t rs, int h, int s, int slot, const u32x8 (&wk)[10], char* lds, int tid) {
    tid = opaque(tid);
    const int lane = tid & 63, w = rfl(tid >> 6), n = lane & 15, g = lane >> 4;
    const int sidx = (n < 8) ? 0x4444 : 0xEEEE;
    f32x4 ak = {0, 0, 0, 0}, ak2 = {0, 0, 0, 0};
#pragma unroll
    for (int kk = 0; kk < 5; ++kk) {
        ak = smfma(lds_ld16(lds + D_MEM + (2 * kk) * 1024 + lane * 16), wk[2 * kk], ak, sidx);
        ak2 = smfma(lds_ld16(lds + D_MEM + (2 * kk + 1) * 1024 + lane * 16), wk[2 * kk + 1], ak2, sidx);
    }
    ak += ak2;
    {
        float k0 = ak[0], k1 = ak[1], k2 = ak[2], k3 = ak[3];
        swap32(k0, k2); swap32(k1, k3);
        const int e = 16 * w + n, ro = ((g & 1) * 4 + (g >> 1) * 2) * 2;
        lds_st4(lds + D_T + e * 32 + 16 + ro, pk_bf16(k0 + k2, k1 + k3));
    }
    __syncthreads();
    if (w < 2) {
        const int qt = w;
        const char* ring = lds + D_RING + slot * 16384;
        f32x4 t0 = {0, 0, 0, 0}, t1 = {0, 0, 0, 0}, t2 = {0, 0, 0, 0};
        const int q4 = n >> 2, p = lane & 3;
#pragma unroll
        for (int t = 0; t < 4; ++t) {
            const char* b = lds + D_T + (32 * t + 8 * g + q4) * 32 + 16 + (p & 1) * 8;
            const u32x2 ka = tr_read(b), kb = tr_read(b + 128);
            const u32x4 kf = {ka.x, ka.y, kb.x, kb.y};
            const int sl = 4 * t + g, rq = 16 * qt + n;
            const u32x4 qf = lds_ld16(ring + rq * 256 + ((sl ^ (rq & 15)) * 16));
            const u32x4 x0 = lds_ld16(ring + 8192 + n * 256 + ((sl ^ n) * 16)), x1 = lds_ld16(ring + 8192 + (16 + n) * 256 + ((sl ^ n) * 16));
            t0 = mfma16(kf, qf, t0); t1 = mfma16(x0, qf, t1); t2 = mfma16(x1, qf, t2);
        }
        const float scale = 0.08838834764831845f, NEG = -1.0e9f;
        const int qi = 16 * qt + n;
        float v[12];
#pragma unroll
        for (int i = 0; i < 4; ++i) { v[i] = t0[i]; v[4 + i] = t1[i]; v[8 + i] = t2[i]; }
#pragma unroll
        for (int i = 0; i < 12; i += 2) {
            u32 a = pk_bf16(v[i], v[i + 1]);
            a = pk_bf16(bf_lo(a) * scale, bf_hi(a) * scale);
            v[i] = bf_lo(a); v[i + 1] = bf_hi(a);
        }
#pragma unroll
        for (int i = 0; i < 4; ++i) {
            v[i] = g < 2 ? v[i] : NEG;
            v[4 + i] = (4 * g + i <= qi) ? v[4 + i] : NEG;
            v[8 + i] = (16 + 4 * g + i <= qi) ? v[8 + i] : NEG;
        }
        float m = vmax3(vmax3(vmax3(v[0], v[1], v[2]), vmax3(v[3], v[4], v[5]), vmax3(v[6], v[7], v[8])), vmax3(v[9], v[10], v[11]), v[0]);
        { float a = m, b = m; swap16(a, b); m = vmax(a, b); }
        { float a = m, b = m; swap32(a, b); m = vmax(a, b); }
        const float ml = -m * 1.4426950408889634f;
        float sum = 0.f;
#pragma unroll
        for (int i = 0; i < 12; ++i) { v[i] = __builtin_amdgcn_exp2f(__builtin_fmaf(v[i], 1.4426950408889634f, ml)); sum += v[i]; }
        { float a = sum, b = sum; swap16(a, b); sum = a + b; }
        { float a = sum, b = sum; swap32(a, b); sum = a + b; }
        const float inv = __builtin_amdgcn_rcpf(sum);
        const u32 pb = WS_PX + s * 81920;
        st16_sc1(rs, pb + (h * 2 + qt) * 1024 + lane * 16,
                 u32x4{pk_bf16(v[4] * inv, v[5] * inv), pk_bf16(v[6] * inv, v[7] * inv), pk_bf16(v[8] * inv, v[9] * inv), pk_bf16(v[10] * inv, v[11] * inv)} & 0x7fff7fffu);
        if (g < 2) st8_sc1(rs, pb + 65536 + ((h >> 2) * 2 + qt) * 1024 + ((h & 3) * 16 + n) * 16 + g * 8, u32x2{pk_bf16(v[0] * inv, v[1] * inv), pk_bf16(v[2] * inv, v[3] * inv)} & 0x7fff7fffu);
    }
}
DEV void role_d(const Params& P, char* ws, gu32* ctl, u32 epoch, int h, int par, char* lds, int tid) {
    u32x8 wq[10], wk[10];
    u32x4 xr[5];
    x_issue(P.x, par, xr, tid);
    load_qk(P.wq, P.wk, h, wq, wk, lds, tid);
    wait_ready(ctl, epoch);
    const rsrc_t rs = mk_rsrc(ws, WS_BYTES);
    u32 parM = 0, parP = 0;
    const int kend = (NSEG - par) / 2, aend = NSEG / 2;
    int px_pending = -1;
    for (int k = -2; k < kend; ++k) {

        int kx_pending = -1;
        if (k + 2 < aend) {
            const int s = par + 2 * (k + 2);
            x_write(lds + D_X, xr, opaque(tid));
            __syncthreads();
            d_ahead(rs, h, s, (k + 2) % 3, wq, wk, lds, tid, px_pending >= 0);
            if (k + 3 < aend) x_issue(P.x, s + 2, xr, opaque(tid));
            if (k < 0 || s == NSEG - 1 || px_pending >= 0) {
                __syncthreads();
                if (px_pending >= 0 && tid == 0) __hip_atomic_fetch_add(cw(ctl, CT_PX + px_pending), 1u, RLX_AGENT);
                px_pending = -1;
            }
            if (s == NSEG - 1) {
                const char* ring = lds + D_RING + ((k + 2) % 3) * 16384;
#pragma unroll
                for (int i = 0; i < 2; ++i) st16_sc1(rs, WS_QK + h * 16384 + (i * 512 + tid) * 16, lds_ld16(ring + (i * 512 + tid) * 16));
            }
            if (k < 0 || s == NSEG - 1) {
                drain();
                __syncthreads();
                if (tid == 0) __hip_atomic_store(cw(ctl, CT_KX + 2 * h + par), (u32)(s + 1), RLX_AGENT);
            } else kx_pending = s + 1;
        }
        if (k == -1) { parM = get_par(ctl, 0); parP = get_par(ctl, 2); wait_ge(ctl, CT_INIT, N_R); }
        if (k >= 0) {
            const int s = par + 2 * k;
            if (s > 0) wait_hint1(rs, ctl, WS_CANP + parP * 8192 + (32 + h) * 128, s + 1, s < NSEG - 3);
            { u32 a_, b_; if (!gather_mem(rs, s, parM, lds + D_MEM, opaque(tid), ctl, a_, b_)) break; }
            drain();
            __syncthreads();
            if (kx_pending >= 0 && tid == 0) __hip_atomic_store(cw(ctl, CT_KX + 2 * h + par), (u32)kx_pending, RLX_AGENT);
            d_behind(rs, h, s, k % 3, wk, lds, tid);
            if (s >= NSEG - 6 || k + 3 >= aend) {
                drain();
                __syncthreads();
                if (tid == 0) __hip_atomic_fetch_add(cw(ctl, CT_PX + s), 1u, RLX_AGENT);
            } else { px_pending = s; __syncthreads(); }
        }
    }
}

DEV void role_a(const Params& P, char* ws, gu32* ctl, u32 epoch, int h, char* lds, int tid) {
    const int lane = tid & 63, w = rfl(tid >> 6), n = lane & 15, g = lane >> 4;
    u32x8 wq[10], wk[10];
    u32x4 xr[5];
    x_issue(P.x, 0, xr, tid);
    load_qk(P.wq, P.wk, h, wq, wk, lds, tid, ws, h);
    const rsrc_t rs = mk_rsrc(ws, WS_BYTES);
    char* mimg = lds;
    char* timg = lds + 10240;
    char* kximg = lds + 16384;
    const int sidx = (n < 8) ? 0x4444 : 0xEEEE;
    {
        x_write(lds + 40960, xr, tid);
        __syncthreads();
        f32x4 ak[2] = {{0, 0, 0, 0}, {0, 0, 0, 0}};
        xgemm<1, true>(lds + 40960, lane, wk, wk, ak, ak);
#pragma unroll
        for (int T = 0; T < 2; ++T)
            lds_st8(kximg + ((T * 4 + (w >> 1)) * 64 + (2 * (w & 1) + (g >> 1)) * 16 + n) * 16 + 8 * (g & 1), u32x2{pk_bf16(ak[T][0], ak[T][1]), pk_bf16(ak[T][2], ak[T][3])});
    }
    wait_ready(ctl, epoch);
    const u32 parM = get_par(ctl, 0), parP = get_par(ctl, 2);
    wait_ge(ctl, CT_INIT, N_R);
    for (int s = 0; s < NSTEP; ++s) {
        u32 nsp_, tg_;
        if (!gather_mem(rs, s, parM, mimg, tid, ctl, nsp_, tg_)) break;
        __syncthreads();
        if (tid == 64) st4_sc1(rs, WS_CANP + parP * 8192 + h * 128, tg_ | (u32)(s + 1));
        f32x4 aq = {0, 0, 0, 0}, ak = {0, 0, 0, 0};
        {
            u32x4 a[10];
#pragma unroll
            for (int kk = 0; kk < 10; ++kk) a[kk] = lds_ld16(mimg + kk * 1024 + lane * 16);
            __builtin_amdgcn_sched_barrier(0);
#pragma unroll
            for (int kk = 0; kk < 10; ++kk) { aq = smfma(a[kk], wq[kk], aq, sidx); ak = smfma(a[kk], wk[kk], ak, sidx); }
            __builtin_amdgcn_sched_barrier(0);
        }
        {
            float a_[4], b_[4];
#pragma unroll
            for (int i = 0; i < 4; ++i) { a_[i] = aq[i]; b_[i] = ak[i]; swap32(a_[i], b_[i]); a_[i] += b_[i]; }
            lds_st8(timg + (16 * w + n) * 32 + (g >> 1) * 16 + (g & 1) * 8, u32x2{pk_bf16(a_[0], a_[1]), pk_bf16(a_[2], a_[3])});
        }
        if (w != 0) __syncthreads();
        else {
            u32x4 x0[4], x1[4];
            const char* kx = kximg + (s & 1) * 8192;
            __builtin_amdgcn_sched_barrier(0);
#pragma unroll
            for (int t = 0; t < 4; ++t) { x0[t] = lds_ld16(kx + (0 * 4 + t) * 1024 + lane * 16); x1[t] = lds_ld16(kx + (1 * 4 + t) * 1024 + lane * 16); }
            __syncthreads();
            f32x4 t0 = {0, 0, 0, 0}, t1 = {0, 0, 0, 0}, t2 = {0, 0, 0, 0};
            const int q4 = n >> 2, p = lane & 3;
            u32x4 qf[4], kf[4];
#pragma unroll
            for (int t = 0; t < 4; ++t) {
                const char* b = timg + (32 * t + 8 * g + q4) * 32 + (p & 1) * 8;
                const u32x2 qa = tr_read(b), qb = tr_read(b + 128);
                qf[t] = u32x4{qa.x, qa.y, qb.x, qb.y};
            }
#pragma unroll
            for (int t = 0; t < 4; ++t) {
                const char* b = timg + (32 * t + 8 * g + q4) * 32 + (p & 1) * 8;
                const u32x2 ka = tr_read(b + 16), kb = tr_read(b + 16 + 128);
                kf[t] = u32x4{ka.x, ka.y, kb.x, kb.y};
            }
            __builtin_amdgcn_sched_barrier(0);
#pragma unroll
            for (int t = 0; t < 4; ++t) { t1 = mfma16(x0[t], qf[t], t1); t2 = mfma16(x1[t], qf[t], t2); }
#pragma unroll
            for (int t = 0; t < 4; ++t) t0 = mfma16(kf[t], qf[t], t0);
            __builtin_amdgcn_sched_barrier(0);
            const bool nhi = n >= 8, ghi = g >= 2;
            const int isel = (n >> 3) + 2 * (g >> 1);
            float v[5];
            { const float a = nhi ? t0[1] : t0[0], b = nhi ? t0[3] : t0[2]; v[0] = ghi ? b : a; }
#pragma unroll
            for (int i = 0; i < 4; ++i) v[1 + i] = nhi ? t2[i] : t1[i];
            const float scale = 0.08838834764831845f;
            {
                u32 a = pk_bf16(v[1], v[2]), b = pk_bf16(v[3], v[4]), c = pk_bf16(v[0], v[0]);
                a = pk_bf16(bf_lo(a) * scale, bf_hi(a) * scale); b = pk_bf16(bf_lo(b) * scale, bf_hi(b) * scale); c = pk_bf16(bf_lo(c) * scale, 0.f);
                v[1] = bf_lo(a); v[2] = bf_hi(a); v[3] = bf_lo(b); v[4] = bf_hi(b); v[0] = bf_lo(c);
            }
            float m = vmax3n(vmax3(v[0], v[1], v[2]), v[3], v[4]);
            m = vmax(m, ror8(m));
            { float a = m, b = m; swap16(a, b); m = vmax(a, b); }
            { float a = m, b = m; swap32(a, b); m = vmax(a, b); }
            const float ml = -m * 1.4426950408889634f;
#pragma unroll
            for (int i = 0; i < 5; ++i) v[i] = __builtin_amdgcn_exp2f(__builtin_fmaf(v[i], 1.4426950408889634f, ml));
            float sum = ((v[0] + v[1]) + (v[2] + v[3])) + v[4];
            sum += ror8(sum);
            { float a = sum, b = sum; swap16(a, b); sum = a + b; }
            { float a = sum, b = sum; swap32(a, b); sum = a + b; }
            const float inv = __builtin_amdgcn_rcpf(sum);
            const int r = n & 7;
            const u32 pb = WS_P + (s % 3) * 49152 + parP * 8192;
            st8_sc1(rs, pb + (h >> 4) * 16384 + (h & 15) * 512 + r * 64 + g * 16 + (n >> 3) * 8,
                    u32x2{pk_bf16(v[1] * inv, v[2] * inv) & 0x7fff7fffu, pk_bf16(v[3] * inv, v[4] * inv) & 0x7fff7fffu});
            st2_sc1(rs, pb + 2 * 16384 + h * 128 + r * 16 + (4 * (g & 1) + isel) * 2, (u16)(pk_bf16(v[0] * inv, 0.f) & 0x7fffu));
        }
        if (s >= 1) {
            const u32 pb = WS_P + ((s + 2) % 3) * 49152 + parP * 8192;
            const u32x4 sv = {SENT, SENT, SENT, SENT};
            if (w == 3 && lane < 32) st16_sc1(rs, pb + (h >> 4) * 16384 + (h & 15) * 512 + lane * 16, sv);
            if (w == 4 && lane < 8) st16_sc1(rs, pb + 2 * 16384 + h * 128 + lane * 16, sv);
        }
        if (s + 1 < NSTEP && w >= 2) {
            wait_ge(ctl, CT_KX + 2 * h + ((s + 1) & 1), s + 2);
            const u32 ko = WS_KX + ((s + 1) * NH + h) * 8192;
            char* kd = kximg + ((s + 1) & 1) * 8192;
            const u32x4 v = ld16_sc1(rs, ko + tid * 16);
            if (w < 4) { const u32x4 v2 = ld16_sc1(rs, ko + (tid - 128) * 16); lds_st16(kd + (tid - 128) * 16, v2); }
            lds_st16(kd + tid * 16, v);
        }
        drain();
    }
    {
        __syncthreads();
        wait_ge(ctl, CT_KX + 2 * h + 1, NSEG);
#pragma unroll
        for (int i = 0; i < 2; ++i) lds_st16(lds + D_RING + (i * 512 + tid) * 16, ld16_sc1(rs, WS_QK + h * 16384 + (i * 512 + tid) * 16));
        u32 a_, b_;
        gather_mem(rs, NSEG - 1, parM, lds + D_MEM, opaque(tid), ctl, a_, b_);
        __syncthreads();
        d_behind(rs, h, NSEG - 1, 0, wk, lds, tid);
    }
}

DEV void role_b(const Params& P, char* ws, gu32* ctl, u32 epoch, int h, char* lds, int tid) {
    const int lane = tid & 63, w = rfl(tid >> 6), n = lane & 15, g = lane >> 4;
    u32x8 wv[10], wo[5][2];
    load_vo(P.wv, P.wo, h, wv, wo, lds, tid);
    const rsrc_t rs = mk_rsrc(ws, WS_BYTES);
    wait_ready(ctl, epoch);
    const u32 parM = get_par(ctl, 0), parZ = get_par(ctl, 1);
    wait_ge(ctl, CT_INIT, N_R);
    char* mimg = lds;
    char* vimg = lds + 10240;
    const int sidx = (n < 8) ? 0x4444 : 0xEEEE;
    const u32 parP = get_par(ctl, 2);
    for (int s = 0; s < NSEG; ++s) {

        u32 nsp_, tg_;
        if (!gather_mem(rs, s, parM, mimg, tid, ctl, nsp_, tg_)) break;
        __syncthreads();
        if (tid == 64) st4_sc1(rs, WS_CANP + parP * 8192 + (32 + h) * 128, tg_ | (u32)(s + 1));
        if (s > 0 && tid == 128) __hip_atomic_fetch_add(cw(ctl, CT_ZP + s - 1), 1u, RLX_AGENT);
        f32x4 av = {0, 0, 0, 0};
        {
            u32x4 a[10];
#pragma unroll
            for (int kk = 0; kk < 10; ++kk) a[kk] = lds_ld16(mimg + kk * 1024 + lane * 16);
            __builtin_amdgcn_sched_barrier(0);
            f32x4 av2 = {0, 0, 0, 0};
#pragma unroll
            for (int kk = 0; kk < 5; ++kk) { av = smfma(a[2 * kk], wv[2 * kk], av, sidx); av2 = smfma(a[2 * kk + 1], wv[2 * kk + 1], av2, sidx); }
            av += av2;
            __builtin_amdgcn_sched_barrier(0);
        }
        {
            float v0 = av[0], v1 = av[1], v2 = av[2], v3 = av[3];
            swap32(v0, v2); swap32(v1, v3);
            const u32 pk = pk_bf16(v0 + v2, v1 + v3);
            const int r0 = (g & 1) * 4 + (g >> 1) * 2;
            char* base = vimg + (w >> 2) * 1024 + ((w & 3) * 16 + ((n >> 1) & 1) * 8) * 16 + (2 * (n >> 2) + (n & 1)) * 2;
            *(LAS u16*)(base + r0 * 16) = (u16)pk;
            *(LAS u16*)(base + (r0 + 1) * 16) = (u16)(pk >> 16);
        }
        __syncthreads();
        const u32x4 a0 = lds_ld16(vimg + lane * 16), a1 = lds_ld16(vimg + 1024 + lane * 16);
        f32x4 d[5];
#pragma unroll
        for (int jt = 0; jt < 5; ++jt) { d[jt] = f32x4{0, 0, 0, 0}; d[jt] = smfma(a0, wo[jt][0], d[jt], sidx); }
#pragma unroll
        for (int jt = 0; jt < 5; ++jt) d[jt] = smfma(a1, wo[jt][1], d[jt], sidx);
        __builtin_amdgcn_sched_barrier(0);
        u32x2 zp[2]; u32 z4;
#pragma unroll
        for (int p = 0; p < 2; ++p) {
            float a_[4], b_[4];
#pragma unroll
            for (int i = 0; i < 4; ++i) { a_[i] = d[2 * p][i]; b_[i] = d[2 * p + 1][i]; swap32(a_[i], b_[i]); a_[i] += b_[i]; }
            zp[p] = u32x2{canon(pk_bf16(a_[0], a_[1])), canon(pk_bf16(a_[2], a_[3]))};
        }
        {
            float v0 = d[4][0], v1 = d[4][1], v2 = d[4][2], v3 = d[4][3];
            swap32(v0, v2); swap32(v1, v3);
            z4 = canon(pk_bf16(v0 + v2, v1 + v3));
        }
        const u32 o8 = (g >> 1) * 16384 + n * 16 + (g & 1) * 8, o4 = n * 16 + ((g & 1) * 2 + (g >> 1)) * 4;
        {
            const u32 zb = WS_Z + (s % 3) * 655360 + parZ * 8192 + h * 256 + 5 * w * 16384;
            st8_sc1(rs, zb + o8, zp[0]); st8_sc1(rs, zb + 2 * 16384 + o8, zp[1]); st4_sc1(rs, zb + 4 * 16384 + o4, z4);
        }
        if (B_STDLY > 0) wait_rt(rt24() + (B_STDLY << 8));
        {
            const u32 zq = WS_ZP + ((s * 40 + 5 * w) * NH + h) * 256;
            const u32 p8 = (g >> 1) * NH * 256 + n * 16 + (g & 1) * 8;
            st8_sc1(rs, zq + p8, zp[0]); st8_sc1(rs, zq + 2 * NH * 256 + p8, zp[1]); st4_sc1(rs, zq + 4 * NH * 256 + o4, z4);
        }
        if (s >= 1 && s + 2 < NSEG) {
            const u32 zs = WS_Z + ((s + 2) % 3) * 655360 + parZ * 8192 + h * 256;
            const u32x4 sv = {SENT, SENT, SENT, SENT};
            st16_sc1(rs, zs + (5 * w + g) * 16384 + n * 16, sv);
            if (lane < 16) st16_sc1(rs, zs + (5 * w + 4) * 16384 + n * 16, sv);
        }
        drain();
    }
    __syncthreads();
    if (tid == 0) __hip_atomic_fetch_add(cw(ctl, CT_ZP + NSEG - 1), 1u, RLX_AGENT);
}

DEV void r_init(const Params& P, gu32* ctl, rsrc_t rs, int j, u32 parM, u32 parZ, u32 parP, int tid) {
    const u32x4 sv = {SENT, SENT, SENT, SENT};
    for (int i = j * 512 + tid; i < NSEG * 640; i += N_R * 512) {
        const int s = i / 640, c = i % 640;
        u32x4 v = sv;
        if (s == 0) {
            const int jj = c >> 4, r16 = c & 15;
            const u16* m = P.mem0 + (r16 & 7) * 640 + 16 * jj + 2 * (r16 >> 3);
            v = u32x4{canon(*(const u32*)(m)), canon(*(const u32*)(m + 4)), canon(*(const u32*)(m + 8)), canon(*(const u32*)(m + 12))};
        }
        st16_sc1(rs, WS_MEM + s * 32768 + (c >> 9) * 16384 + parM * 8192 + (c & 511) * 16, v);
    }
    for (int i = j * 512 + tid; i < 3 * 40 * 512; i += N_R * 512) st16_sc1(rs, WS_Z + (i >> 9) * 16384 + parZ * 8192 + (i & 511) * 16, sv);
    for (int i = j * 512 + tid; i < 3 * 3 * 512; i += N_R * 512) st16_sc1(rs, WS_P + (i >> 9) * 16384 + parP * 8192 + (i & 511) * 16, sv);
    for (int i = j * 512 + tid; i < 2 * 81920 / 16; i += N_R * 512) st16_sc1(rs, WS_PX + (NSEG - 2) * 81920 + i * 16, sv);
    if (tid == 0 && j < N_A) { st4_sc1(rs, WS_CANP + parP * 8192 + j * 128, 0); st4_sc1(rs, WS_CANP + parP * 8192 + (32 + j) * 128, 0); }
    drain();
    __syncthreads();
    if (tid == 0) __hip_atomic_fetch_add(cw(ctl, CT_INIT), 1u, RLX_AGENT);
}
DEV void role_r(const Params& P, char* ws, gu32* ctl, u32 epoch, int j, char* lds, int tid) {
    const int lane = tid & 63, w = rfl(tid >> 6), n = lane & 15, g = lane >> 4;
    const rsrc_t rs = mk_rsrc(ws, WS_BYTES);
    if (j < 3) calibrate(ws, ctl, epoch, j, lds, tid);
    wait_ready(ctl, epoch);
    const u32 parM = get_par(ctl, 0), parZ = get_par(ctl, 1), parP = get_par(ctl, 2);
    r_init(P, ctl, rs, j, parM, parZ, parP, tid);
    wait_ge(ctl, CT_INIT, N_R);
    u32 tpub = 0;
    for (int s = 0; s < NSTEP; ++s) {

        if (R_VWDLY > 0 && s > 0) wait_rt(tpub + (R_VWDLY << 8));
        wait_ge(ctl, CT_VW + s, N_C);
        u32x4 vw[4];
#pragma unroll
        for (int i = 0; i < 4; ++i) vw[i] = ld16_sc1(rs, WS_VW + ((s * 40 + j) * NH + 4 * w + i) * 1024 + lane * 16);
        bool ok = true;
        u32x4 z, px0, px1, pm = {0, 0, 0, 0};
        const u32 oz = WS_Z + (s % 3) * 655360 + j * 16384 + parZ * 8192 + (4 * w + g) * 256 + n * 16;
        const u32 pb = WS_P + (s % 3) * 49152 + parP * 8192;
        const int hA = 4 * w + (n >> 3);
        const u32 ox0 = pb + (hA >> 4) * 16384 + (hA & 15) * 512 + (n & 7) * 64 + g * 16;
        const u32 om = pb + 2 * 16384 + (4 * w + g) * 128 + (n & 7) * 16;
        bool zbad = true, pbad = true;
        u32 spins = 0;
        if (DLY_R > 0) {
            u32 tz;
            const u32 tp = wait_hints8(rs, ctl, WS_CANP + parP * 8192, 4 * w, s + 1, lane, DLY_R << 8, DLY_B << 8, tz);
            wait_rt((int)(tz - tp) > 0 ? tz : tp);
        } else {
            for (;;) { z = ld16_sc1(rs, oz); zbad = __any(is_sent(z)); if (!zbad) break; if (spin_abort(spins, ctl)) { ok = false; break; } }
        }
        u32 nsp_ = 0;
        for (;;) {
            if (zbad) z = ld16_sc1(rs, oz);
            if (pbad) { px0 = ld16_sc1(rs, ox0); px1 = ld16_sc1(rs, ox0 + 1024); pm = ld16_sc1(rs, om); }
            if (zbad) zbad = __any(is_sent(z));
            if (pbad) pbad = __any(any_sign(px0) | any_sign(px1) | any_sign(pm));
            if (!(zbad | pbad)) break;
            nsp_ += (pbad ? 1 : 0) + (zbad ? 1000 : 0);
            if (spin_abort(spins, ctl)) { ok = false; break; }
        }
        f32x4 aL = {0, 0, 0, 0}, aH = {0, 0, 0, 0};
        aL = mfma16(z, pm, aL);
        aL = mfma16(vw[0], px0, aL); aH = mfma16(vw[1], px0, aH);
        aL = mfma16(vw[2], px1, aL); aH = mfma16(vw[3], px1, aH);
        f32x4 part;
#pragma unroll
        for (int i = 0; i < 4; ++i) part[i] = aL[i] + ror8(aH[i]);
        if (n < 8) *(LAS f32x4*)(lds + w * 512 + (g * 8 + n) * 16) = part;
        __syncthreads();
        if (w == 0) {
            const int r16 = lane >> 2, q = lane & 3, r = r16 & 7, hi = r16 >> 3;
            f32x2 t[8];
#pragma unroll
            for (int ww = 0; ww < 8; ++ww) t[ww] = *(const LAS f32x2*)(lds + ww * 512 + (q * 8 + r) * 16 + hi * 8);
            const f32x2 sm = ((t[0] + t[1]) + (t[2] + t[3])) + ((t[4] + t[5]) + (t[6] + t[7]));
            const float s0 = sm.x, s1 = sm.y;
            const int c = 16 * j + r16;
            st4_sc1(rs, WS_MEM + (s + 1) * 32768 + (c >> 9) * 16384 + parM * 8192 + (c & 511) * 16 + q * 4, canon(pk_bf16(s0, s1)));
        }
        if (R_VWDLY > 0) tpub = rt24();
        __syncthreads();
        if (!ok) break;
    }
}

DEV void f_task(const Params& P, rsrc_t rs, gu32* ctl, int s, int jg, char* lds, int tid) {
    const int lane = tid & 63, w = rfl(tid >> 6), n = lane & 15, g = lane >> 4;
    const int j = 8 * jg + w;
    wait_ge_slow(ctl, CT_VW + s, N_C);
    const u32 vb = WS_VW + (u32)(s * 40 + j) * NH * 1024 + lane * 16, zb = WS_ZP + (u32)(s * 40 + j) * NH * 256 + lane * 16;
    u32x4 a[32], z[8];
#pragma unroll
    for (int i = 0; i < 32; ++i) {
        a[i] = ld16_sc1(rs, vb + i * 1024);
        if ((i % F_PACE) == F_PACE - 1) { drain(); __builtin_amdgcn_s_sleep(F_SLEEP); }
    }
    if (w == 0) { if (s < NSEG - 6) wait_ge_slow(ctl, CT_PX + s, NH); else wait_ge(ctl, CT_PX + s, NH); }
    __syncthreads();
    {
        u32x4 t[10];
#pragma unroll
        for (int i = 0; i < 10; ++i) t[i] = ld16_sc1(rs, WS_PX + s * 81920 + (i * 512 + tid) * 16);
        wait_ge(ctl, CT_ZP + s, N_B);
#pragma unroll
        for (int i = 0; i < 8; ++i) z[i] = ld16_sc1(rs, zb + i * 1024);
#pragma unroll
        for (int i = 0; i < 10; ++i) lds_st16(lds + (i * 512 + tid) * 16, t[i]);
    }
    __syncthreads();
    f32x4 d0 = {0, 0, 0, 0}, d1 = {0, 0, 0, 0};
    {
        u32x4 b[2][4];
        auto frag = [&](int k, int qt) { return lds_ld16(lds + (k < 32 ? (k * 2 + qt) * 1024 : 65536 + ((k - 32) * 2 + qt) * 1024) + lane * 16); };
#pragma unroll
        for (int i = 0; i < 4; ++i) b[0][i] = frag(i >> 1, i & 1);
#pragma unroll
        for (int g2 = 0; g2 < 20; ++g2) {
            if (g2 < 19) {
#pragma unroll
                for (int i = 0; i < 4; ++i) b[(g2 + 1) & 1][i] = frag(2 * (g2 + 1) + (i >> 1), i & 1);
            }
            __builtin_amdgcn_sched_barrier(0);
            const u32x4 a0 = 2 * g2 < 32 ? a[2 * g2 < 32 ? 2 * g2 : 0] : z[2 * g2 >= 32 ? 2 * g2 - 32 : 0], a1 = 2 * g2 + 1 < 32 ? a[2 * g2 + 1 < 32 ? 2 * g2 + 1 : 0] : z[2 * g2 + 1 >= 32 ? 2 * g2 + 1 - 32 : 0];
            d0 = mfma16(a0, b[g2 & 1][0], d0); d1 = mfma16(a0, b[g2 & 1][1], d1);
            d0 = mfma16(a1, b[g2 & 1][2], d0); d1 = mfma16(a1, b[g2 & 1][3], d1);
            __builtin_amdgcn_sched_barrier(0);
        }
    }
    char* o = lds + 81920;
    lds_st8(o + n * 272 + w * 32 + g * 8, u32x2{pk_bf16(d0[0], d0[1]), pk_bf16(d0[2], d0[3])});
    lds_st8(o + (16 + n) * 272 + w * 32 + g * 8, u32x2{pk_bf16(d1[0], d1[1]), pk_bf16(d1[2], d1[3])});
    __syncthreads();
    { const int row = tid >> 4, c = tid & 15;
      *(GAS u32x4*)(P.out + (size_t)(32 * s + row) * OUTW + 640 + 128 * jg + c * 8) = lds_ld16(o + row * 272 + c * 16); }
    __syncthreads();
}

DEV void f_fine(const Params& P, rsrc_t rs, gu32* ctl, int s, int j, char* lds, int tid) {
    const int lane = tid & 63, w = rfl(tid >> 6);
    if (w == 0) { wait_ge_slow(ctl, CT_VW + s, N_C); wait_ge_slow(ctl, CT_ZP + s - 3, N_B); }
    __syncthreads();
    u32x4 vw[4];
#pragma unroll
    for (int i = 0; i < 4; ++i) vw[i] = ld16_sc1(rs, WS_VW + ((s * 40 + j) * NH + 4 * w + i) * 1024 + lane * 16);
    u32x4 px[4][2], pm[2], z;
    const u32 pb = WS_PX + s * 81920 + lane * 16;
    const u32 oz = WS_Z + (s % 3) * 655360 + j * 16384 + get_par(ctl, 1) * 8192 + 4 * w * 256 + lane * 16;
    {
        const u32 ho = WS_CANP + get_par(ctl, 2) * 8192 + (32 + 4 * w) * 128;
        wait_hint1(rs, ctl, ho, s, true);
        const u32 th = wait_hint1(rs, ctl, ho, s + 1, false);
        wait_rt((th & ~255u) + ((s == NSEG - 1 ? TAIL_DELAY : TAIL_DELAY2) << 8));
        u32 spins = 0;
        for (;;) {
#pragma unroll
            for (int i = 0; i < 4; ++i) { px[i][0] = ld16_sc1(rs, pb + ((4 * w + i) * 2 + 0) * 1024); px[i][1] = ld16_sc1(rs, pb + ((4 * w + i) * 2 + 1) * 1024); }
            pm[0] = ld16_sc1(rs, pb + 65536 + (w * 2 + 0) * 1024); pm[1] = ld16_sc1(rs, pb + 65536 + (w * 2 + 1) * 1024);
            z = ld16_sc1(rs, oz);
            bool bad = is_sent(z) | any_sign(pm[0]) | any_sign(pm[1]);
#pragma unroll
            for (int i = 0; i < 4; ++i) bad |= any_sign(px[i][0]) | any_sign(px[i][1]);
            if (!__any(bad)) break;
            if (spin_abort(spins, ctl)) break;
        }
    }
    f32x4 d0 = {0, 0, 0, 0}, d1 = {0, 0, 0, 0};
#pragma unroll
    for (int i = 0; i < 4; ++i) { d0 = mfma16(vw[i], px[i][0], d0); d1 = mfma16(vw[i], px[i][1], d1); }
    d0 = mfma16(z, pm[0], d0); d1 = mfma16(z, pm[1], d1);
    *(LAS f32x4*)(lds + w * 2048 + lane * 16) = d0;
    *(LAS f32x4*)(lds + w * 2048 + 1024 + lane * 16) = d1;
    __syncthreads();
    if (tid < 128) {
        f32x4 a = {0, 0, 0, 0};
#pragma unroll
        for (int ww = 0; ww < 8; ++ww) a += *(const LAS f32x4*)(lds + ww * 2048 + w * 1024 + lane * 16);
        *(GAS u32x2*)(P.out + (size_t)(32 * s + 16 * w + (lane & 15)) * OUTW + 640 + 16 * j + 4 * (lane >> 4)) = u32x2{pk_bf16(a[0], a[1]), pk_bf16(a[2], a[3])};
    }
    __syncthreads();
}
struct cf { float r, i; };
DEV cf cmul(cf a, cf b) { return cf{__builtin_fmaf(a.r, b.r, -a.i * b.i), __builtin_fmaf(a.r, b.i, a.i * b.r)}; }
DEV cf cexp_k(float lr, float rh, float rl, float k) {
    const float mag = __builtin_amdgcn_exp2f(lr * k * 1.4426950408889634f);
    const float fr = __builtin_amdgcn_fractf(__builtin_amdgcn_fractf(rh * k) + rl * k);
    return cf{mag * __builtin_amdgcn_cosf(fr), mag * __builtin_amdgcn_sinf(fr)};
}
constexpr int A_WR = 16896;
constexpr int A_ROW = 528;
constexpr int A_TAB = 0;
constexpr int A_KC0 = 3072;
constexpr int A_KC1 = 3072 + 4224 + 64;
constexpr int A_UT = 3072 + 4224 + 64 + 4224;
constexpr int A_ZERO = A_UT + 4608;
static_assert(A_ZERO + 128 <= A_WR, "part A region");

DEV void parta_task(const Params& P, int task, char* lds, int tid) {
    const int lane = tid & 63, w = __builtin_amdgcn_readfirstlane(tid >> 6);
    const int d0 = task * 8, d = d0 + w;
    char* reg = lds + w * A_WR;
    u32x4 ur[4];
#pragma unroll
    for (int i = 0; i < 4; ++i) ur[i] = *(const GAS u32x4*)(P.u + (size_t)(i * 512 + tid) * DM + d0);
    const float dt = rbf(expf(bf2f(P.logdt[d])));
    f32x4 acc[3] = {{0, 0, 0, 0}, {0, 0, 0, 0}, {0, 0, 0, 0}};
    const int fa = lane & 15, kq = lane >> 4;
#pragma unroll 1
    for (int half = 0; half < 2; ++half) {
        const int n = d * NST + half * 64 + lane;
        const float lr = dt * bf2f(P.are[n]), li = dt * bf2f(P.aim[n]);
        const float br = dt * bf2f(P.bre[n]), bi = dt * bf2f(P.bim[n]);
        const cf W = cmul(cf{bf2f(P.cre[n]), bf2f(P.cim[n])}, cf{br, bi});
        const float rh = li * 0.15915494309189535f;
        const float rl = __builtin_fmaf(li, 0.15915494309189535f, -rh) + li * 6.4206383167e-9f;
        const cf E1 = cexp_k(lr, rh, rl, 1.f), E8 = cexp_k(lr, rh, rl, 8.f), E128 = cexp_k(lr, rh, rl, 128.f);
        char* Lt = reg; char* Rt = reg + 16 * A_ROW;
        {
            cf r = {1.f, 0.f};
#pragma unroll
            for (int j = 0; j < 16; ++j) { *(LAS f32x2*)(Rt + j * A_ROW + lane * 8) = f32x2{r.r, -r.i}; r = cmul(r, E8); }
        }
        cf C[8]; C[0] = cf{1.f, 0.f};
#pragma unroll
        for (int c = 1; c < 8; ++c) C[c] = cmul(C[c - 1], E1);
        f32x4 rb[8];
#pragma unroll
        for (int G = 0; G < 8; ++G) rb[G] = *(const LAS f32x4*)(Rt + fa * A_ROW + (8 * G + 2 * kq) * 8);
        cf WB = W;
#pragma unroll
        for (int T = 0; T < 3; ++T) {
            if (T == 0) {
#pragma unroll
                for (int a = 0; a < 2; ++a) {
#pragma unroll
                    for (int c = 0; c < 8; ++c) { const cf v = c ? cmul(WB, C[c]) : WB; *(LAS f32x2*)(Lt + (8 * a + c) * A_ROW + lane * 8) = f32x2{v.r, v.i}; }
                    WB = cmul(WB, E128);
                }
            } else if (T == 1) {
#pragma unroll
                for (int a = 0; a < 2; ++a) {
#pragma unroll
                    for (int c = 0; c < 4; ++c) { const cf v = c ? cmul(WB, C[2 * c]) : WB; *(LAS f32x2*)(Lt + (4 * a + c) * A_ROW + lane * 8) = f32x2{v.r, v.i}; }
                    WB = cmul(WB, E128);
                }
#pragma unroll
                for (int a = 0; a < 4; ++a) {
#pragma unroll
                    for (int c = 0; c < 2; ++c) { const cf v = c ? cmul(WB, C[4]) : WB; *(LAS f32x2*)(Lt + (8 + 2 * a + c) * A_ROW + lane * 8) = f32x2{v.r, v.i}; }
                    WB = cmul(WB, E128);
                }
            } else {
#pragma unroll
                for (int a = 0; a < 9; ++a) { *(LAS f32x2*)(Lt + a * A_ROW + lane * 8) = f32x2{WB.r, WB.i}; WB = cmul(WB, E128); }
#pragma unroll
                for (int a = 9; a < 16; ++a) *(LAS f32x2*)(Lt + a * A_ROW + lane * 8) = f32x2{0.f, 0.f};
            }
#pragma unroll
            for (int G = 0; G < 8; ++G) {
                const f32x4 la = *(const LAS f32x4*)(Lt + fa * A_ROW + (8 * G + 2 * kq) * 8);
#pragma unroll
                for (int e = 0; e < 4; ++e) acc[T] = __builtin_amdgcn_mfma_f32_16x16x4f32(la[e], rb[G][e], acc[T], 0, 0, 0);
            }
        }
    }
#pragma unroll
    for (int T = 0; T < 3; ++T)
#pragma unroll
        for (int i = 0; i < 4; ++i) *(LAS float*)(reg + A_TAB + ((16 * T + 4 * kq + i) * 16 + fa) * 4) = acc[T][i];
    auto kval = [&](int l) -> float {
        const int lb = (int)rbf((float)l);
        const int sh = min(max(24 - (int)__builtin_clz((unsigned)lb | 1u), 0), 3);
        const int row = 8 * sh + ((lb >> 7) << (3 - sh)) + ((lb & 7) >> sh);
        return *(const LAS float*)(reg + A_TAB + (row * 16 + ((lb >> 3) & 15)) * 4);
    };
#pragma unroll 4
    for (int it = 0; it < 16; ++it) {
        const int p = it * 64 + lane, l0 = 2047 - 2 * p;
        lds_st4(reg + A_KC0 + p * 4, pk_bf16(kval(l0), kval(l0 - 1)));
    }
    if (lane < 33) lds_st4(reg + A_KC0 + (1024 + lane) * 4, 0u);
#pragma unroll 4
    for (int it = 0; it < 17; ++it) {
        const int p = min(it * 64 + lane, 1055);
        const u32 a = *(const LAS u32*)(reg + A_KC0 + p * 4), b2 = *(const LAS u32*)(reg + A_KC0 + p * 4 + 4);
        lds_st4(reg + A_KC1 + p * 4, __builtin_amdgcn_alignbit(b2, a, 16));
    }
    if (lane < 32) lds_st4(reg + A_ZERO + lane * 4, 0u);
    __syncthreads();
#pragma unroll
    for (int i = 0; i < 4; ++i) {
        const int t = i * 512 + tid;
#pragma unroll
        for (int c = 0; c < 4; ++c) {
            *(LAS u16*)(lds + (2 * c) * A_WR + A_UT + (t >> 6) * 144 + (t & 63) * 2) = (u16)ur[i][c];
            *(LAS u16*)(lds + (2 * c + 1) * A_WR + A_UT + (t >> 6) * 144 + (t & 63) * 2) = (u16)(ur[i][c] >> 16);
        }
    }
    __syncthreads();
    f32x16 y0, y1;
#pragma unroll
    for (int i = 0; i < 16; ++i) { y0[i] = 0.f; y1[i] = 0.f; }
    const int b = lane & 31, hh = lane >> 5;
    const char* kbase = reg + ((b & 1) ? A_KC0 + (2047 - b + 8 * hh) * 2 : A_KC1 + (2046 - b + 8 * hh) * 2);
    auto kfrag = [&](int m) -> u32x4 { const char* p = kbase - 32 * m; u32x4 r; r.x = *(const LAS u32*)(p); r.y = *(const LAS u32*)(p + 4); r.z = *(const LAS u32*)(p + 8); r.w = *(const LAS u32*)(p + 12); return r; };
    u32x4 Fm1 = kfrag(-1), Fm2 = kfrag(-2), Fm3 = kfrag(-3);
#pragma unroll 2
    for (int dl = 0; dl < 32; ++dl) {
        const u32x4 F0 = kfrag(4 * dl), F1 = kfrag(4 * dl + 1), F2 = kfrag(4 * dl + 2);
        const char* ub = (b >= dl) ? reg + A_UT + (b - dl) * 144 + 16 * hh : reg + A_ZERO;
        const int ks = (b >= dl) ? 32 : 0;
        const u32x4 U0 = lds_ld16(ub), U1 = lds_ld16(ub + ks), U2 = lds_ld16(ub + 2 * ks), U3 = lds_ld16(ub + 3 * ks);
        y0 = mfma32(F0, U0, y0); y1 = mfma32(F2, U0, y1);
        y0 = mfma32(Fm1, U1, y0); y1 = mfma32(F1, U1, y1);
        y0 = mfma32(Fm2, U2, y0); y1 = mfma32(F0, U2, y1);
        y0 = mfma32(Fm3, U3, y0); y1 = mfma32(Fm1, U3, y1);
        Fm1 = kfrag(4 * dl + 3); Fm2 = F2; Fm3 = F1;
    }
#pragma unroll
    for (int q = 0; q < 4; ++q) {
        lds_st8(reg + b * 144 + (8 * q + 4 * hh) * 2, u32x2{pk_bf16(y0[4 * q], y0[4 * q + 1]), pk_bf16(y0[4 * q + 2], y0[4 * q + 3])});
        lds_st8(reg + b * 144 + (32 + 8 * q + 4 * hh) * 2, u32x2{pk_bf16(y1[4 * q], y1[4 * q + 1]), pk_bf16(y1[4 * q + 2], y1[4 * q + 3])});
    }
    __syncthreads();
    const u32x4 dsk = *(const GAS u32x4*)(P.dskip + d0);
#pragma unroll
    for (int i = 0; i < 4; ++i) {
        const int t = i * 512 + tid;
        u32x4 yv;
#pragma unroll
        for (int c = 0; c < 4; ++c) {
            const u32 lo = *(const LAS u16*)(lds + (2 * c) * A_WR + (t >> 6) * 144 + (t & 63) * 2), hi = *(const LAS u16*)(lds + (2 * c + 1) * A_WR + (t >> 6) * 144 + (t & 63) * 2);
            yv[c] = lo | (hi << 16);
        }
        u32x4 o;
#pragma unroll
        for (int c = 0; c < 4; ++c) {
            const u32 sk = pk_bf16(bf_lo(ur[i][c]) * bf_lo(dsk[c]), bf_hi(ur[i][c]) * bf_hi(dsk[c]));
            o[c] = pk_bf16(bf_lo(yv[c]) + bf_lo(sk), bf_hi(yv[c]) + bf_hi(sk));
        }
        *(GAS u32x4*)(P.out + (size_t)t * OUTW + d0) = o;
    }
    __syncthreads();
}

constexpr int F_FINE0 = NSEG - 2;

#define PAT_ARG
#define PAT_DECL constexpr int na[4] = {8, 8, 8, 8}, nb[4] = {8, 8, 8, 8}, nr[4] = {10, 10, 10, 10};
DEV int slot_role(int x, int t PAT_ARG) {
    if (x >= 4) {
        const int xx = x - 4;
        return t < 8 ? N_HALF0 + 8 * xx + t : t < 24 ? N_HALF0 + N_C + 8 * xx + ((t - 8) & 7) + 32 * ((t - 8) >> 3) : N_HALF0 + N_HALF1 + 8 * xx + (t - 24);
    }
    PAT_DECL
    int a0 = 0, b0 = 0, r0 = 0, w0 = 0;
#pragma unroll
    for (int i = 0; i < 4; ++i) if (i < x) { a0 += na[i]; b0 += nb[i]; r0 += nr[i]; w0 += 32 - na[i] - nb[i] - nr[i]; }
    int ca = na[0], cb = nb[0], cr = nr[0];
#pragma unroll
    for (int i = 1; i < 4; ++i) if (i == x) { ca = na[i]; cb = nb[i]; cr = nr[i]; }
    return t < ca ? a0 + t : t < ca + cb ? N_A + b0 + (t - ca) : t < ca + cb + cr ? N_A + N_B + r0 + (t - ca - cb) : N_HALF0 + N_HALF1 + 32 + w0 + (t - ca - cb - cr);
}

__global__ void __launch_bounds__(512, 2) concat_heads_mega(Params P) {
    extern __shared__ __attribute__((aligned(16))) char lds[];
    char* ws = ws_base(P.ws);
    gu32* ctl = (gu32*)P.ws;
    const int tid = threadIdx.x;
    if (tid == 0) {
        const u32 ep = __hip_atomic_load(cw(ctl, CT_READY), RLX_AGENT) + 1u;
        const int x0 = (int)((unsigned)__builtin_amdgcn_s_getreg(20 | (3 << 11)) & 7u), t0 = (int)(blockIdx.x >> 3) & 31;
        int x = x0, t = t0;
        for (int k = 0; k < 256; ++k) {
            x = (x0 + (k >> 5)) & 7; t = (t0 + k) & 31;
            if (__hip_atomic_exchange(cw(ctl, CT_SLOT + x * 32 + t), ep, RLX_AGENT) != ep) break;
        }
        *(LAS int*)lds = slot_role(x, t);
        *(LAS u32*)(lds + 4) = ep; *(LAS int*)(lds + 8) = x == 0 && t == 31;
    }
    __syncthreads();
    const int role = rfl(*(LAS int*)lds);
    const u32 epoch = (u32)rfl(*(LAS int*)(lds + 4));
    const bool clears = rfl(*(LAS int*)(lds + 8));
    __syncthreads();
    if (clears) {
        if (tid < CT_LINES) __hip_atomic_store(cw(ctl, tid), 0u, RLX_AGENT);
        if (tid < 256) { u32 spins = 0; while (__hip_atomic_load(cw(ctl, CT_SLOT + tid), RLX_AGENT) != epoch && ++spins < SPIN_CAP) __builtin_amdgcn_s_sleep(1); }
        drain();
        __syncthreads();
        if (tid == 0) __hip_atomic_store(cw(ctl, CT_READY), epoch, RLX_AGENT);
    }
    if (role < N_A) role_a(P, ws, ctl, epoch, role, lds, tid);
    else if (role < N_A + N_B) role_b(P, ws, ctl, epoch, role - N_A, lds, tid);
    else if (role < N_HALF0) {
        role_r(P, ws, ctl, epoch, role - N_A - N_B, lds, tid);
        f_fine(P, mk_rsrc(ws, WS_BYTES), ctl, NSEG - 1, role - N_A - N_B, lds, tid);
    }
    else if (role < N_HALF0 + N_C) {
        role_c(P, ws, ctl, epoch, role - N_HALF0, lds, tid);
        f_fine(P, mk_rsrc(ws, WS_BYTES), ctl, NSEG - 2, role - N_HALF0, lds, tid);
    } else if (role < N_HALF0 + N_HALF1) {
        const int d = role - N_HALF0 - N_C;
        role_d(P, ws, ctl, epoch, d & 31, d >> 5, lds, tid);
        if (d >= 32 && (d & 7) < 2) f_fine(P, mk_rsrc(ws, WS_BYTES), ctl, NSEG - 2, 32 + ((d - 32) >> 3) * 2 + (d & 7), lds, tid);
    }
    else {
        const int wk = role - N_HALF0 - N_HALF1;
        const bool far_worker = wk < 32;
        if (W_DELAY > 0) wait_rt(rt24() + (W_DELAY << 8));
        wait_ready(ctl, epoch);
        {
            parta_task(P, wk, lds, tid);
            if (!far_worker) parta_task(P, wk + 24, lds, tid);
        }
        const rsrc_t rs = mk_rsrc(ws, WS_BYTES);
        {
            if (far_worker) {
                for (;;) {
                    if (tid == 0) *(LAS int*)(lds + LDS_SLOT) = (int)__hip_atomic_fetch_add(cw(ctl, CT_FQ), 1u, RLX_AGENT);
                    __syncthreads();
                    const int t = rfl(*(LAS int*)(lds + LDS_SLOT));
                    if (t >= F_FINE0 * 5) break;
                    f_task(P, rs, ctl, t / 5, t % 5, lds, tid);
                }
                __syncthreads();
            }
        }
    }
}

extern "C" void kernel_launch(void* const* d_in, const int* in_sizes, int n_in, void* d_out, int out_size, void* d_ws, size_t ws_size, hipStream_t stream) {
    Params P;
    P.u = (const u16*)d_in[0]; P.are = (const u16*)d_in[1]; P.aim = (const u16*)d_in[2]; P.bre = (const u16*)d_in[3]; P.bim = (const u16*)d_in[4];
    P.cre = (const u16*)d_in[5]; P.cim = (const u16*)d_in[6]; P.logdt = (const u16*)d_in[7]; P.dskip = (const u16*)d_in[8];
    P.x = (const u16*)d_in[9]; P.mem0 = (const u16*)d_in[10]; P.wq = (const u16*)d_in[11]; P.wk = (const u16*)d_in[12]; P.wv = (const u16*)d_in[13]; P.wo = (const u16*)d_in[14];
    P.out = (u16*)d_out; P.ws = (char*)d_ws;
    (void)hipFuncSetAttribute((const void*)concat_heads_mega, hipFuncAttributeMaxDynamicSharedMemorySize, LDS_BYTES);
    hipLaunchKernelGGL(concat_heads_mega, dim3(N_WG), dim3(512), LDS_BYTES, stream, P);
}
```
